```python
import torch

def _softmax(x, axis=-1):
    return torch.softmax(x, dim=axis)

def _gelu(x):
    return torch.nn.functional.gelu(x, approximate='tanh')

def _silu(x):
    return torch.nn.functional.silu(x)

def _sigmoid(x):
    return torch.sigmoid(x)

def _layernorm(x, axis=-1, eps=1e-05):
    m = x.mean(dim=axis, keepdim=True)
    v = x.var(dim=axis, keepdim=True, unbiased=False)
    return (x - m) / torch.sqrt(v + eps)

def _scatter_add(tgt, idx, src, axis=0):
    return tgt.clone().index_add_(axis, idx, src)

def _segment_sum(src, seg, n):
    out = torch.zeros((n,) + src.shape[1:], dtype=src.dtype)
    return out.index_add_(0, seg, src)

def _pad(x, pw):
    flat = tuple((v for pair in reversed(pw) for v in pair))
    return torch.nn.functional.pad(x, flat)
kernel_name = 'parallel_sum__memory_aug_recurrent_mem_tr__dense_activation_relu__prompt'

def setup_inputs():

    _w = lambda a: (lambda t: t.to(torch.bfloat16) if t.is_floating_point() else t)(torch.as_tensor(a))

    def _A_setup():
        B, S, Dm = (1, 2048, 512)
        H, Dh = (32, 128)
        M, seg = (8, 32)
        seg = min(seg, S)
        S = S // seg * seg
        return {'x': _w((lambda *s: torch.randn(*s, dtype=torch.bfloat16) * 0.05)(B, S, Dm)), 'mem0': _w((lambda *s: torch.randn(*s, dtype=torch.bfloat16) * 1.6)(B, M, Dm)), 'Wq': _w((lambda *s: torch.randn(*s, dtype=torch.bfloat16) * 0.8)(Dm, H, Dh) * Dm ** (-0.5)), 'Wk': _w((lambda *s: torch.randn(*s, dtype=torch.bfloat16) * 0.8)(Dm, H, Dh) * Dm ** (-0.5)), 'Wv': _w((lambda *s: torch.randn(*s, dtype=torch.bfloat16) * 1.6)(Dm, H, Dh) * Dm ** (-0.5)), 'Wo': _w((lambda *s: torch.randn(*s, dtype=torch.bfloat16) * 1.6)(H, Dh, Dm) * (H * Dh) ** (-0.5)), 'seg_len': seg}
    _A_in = _A_setup()

    def _B_setup():
        B, S, D, Dff = (1, 2048, 512, 1376)
        return {'x': _w((lambda *s: torch.randn(*s, dtype=torch.bfloat16) * 0.05)(B, S, D)), 'w1': _w((lambda *s: torch.randn(*s, dtype=torch.bfloat16) * 0.05)(D, Dff)), 'w2': _w((lambda *s: torch.randn(*s, dtype=torch.bfloat16) * 0.05)(Dff, D))}
    _B_in = _B_setup()
    return {'A_x': _A_in['x'], 'A_mem0': _A_in['mem0'], 'A_Wq': _A_in['Wq'], 'A_Wk': _A_in['Wk'], 'A_Wv': _A_in['Wv'], 'A_Wo': _A_in['Wo'], 'A_seg_len': _A_in['seg_len'], 'B_x': _B_in['x'], 'B_w1': _B_in['w1'], 'B_w2': _B_in['w2']}

def reference(A_x, A_mem0, A_Wq, A_Wk, A_Wv, A_Wo, A_seg_len, B_x, B_w1, B_w2):

    def _A_ref(x, mem0, Wq, Wk, Wv, Wo, seg_len):
        B, S, Dm = x.shape
        H, Dh = (Wq.shape[1], Wq.shape[2])
        M = mem0.shape[1]
        scale = Dh ** (-0.5)
        n_seg = S // seg_len
        L = M + seg_len
        idx = torch.arange(L)
        mask = (idx[:, None] < M) | (idx[:, None] >= idx[None, :])
        mem = mem0
        outs = []
        for s in range(n_seg):
            xs = x[:, s * seg_len:(s + 1) * seg_len, :].clone()
            h = torch.concatenate([mem, xs], axis=1)
            q = torch.einsum('btd,dhe->bhte', h, Wq)
            k = torch.einsum('btd,dhe->bhte', h, Wk)
            v = torch.einsum('btd,dhe->bhte', h, Wv)
            scores = torch.einsum('bhqe,bhke->bhqk', q, k) * scale
            scores = torch.where(mask[None, None], scores, -1000000000.0)
            p = _softmax(scores, axis=-1)
            o = torch.einsum('bhqk,bhke->bhqe', p, v)
            o = torch.einsum('bhte,hed->btd', o, Wo)
            mem = o[:, :M, :].clone()
            outs.append(o[:, M:, :])
        return torch.concatenate(outs, axis=1)

    def _B_ref(x, w1, w2):
        h = torch.einsum('bsd,df->bsf', x, w1)
        zero = torch.zeros_like(h)
        h = torch.maximum(h, zero)
        return torch.einsum('bsf,fd->bsd', h, w2)
    x = A_x
    xn = _layernorm(x, axis=-1)
    return x + _A_ref(xn, A_mem0, A_Wq, A_Wk, A_Wv, A_Wo, A_seg_len) + _B_ref(xn, B_w1, B_w2)
```

```cpp
#include <hip/hip_runtime.h>
#include <hip/hip_bf16.h>
#include <stdint.h>

typedef unsigned short u16;
__device__ __forceinline__ float bf2f(u16 v) { return __uint_as_float(((unsigned)v) << 16); }
__device__ __forceinline__ u16 f2bf(float f) { __hip_bfloat16 b = __float2bfloat16(f); return *(u16*)&b; }
__device__ __forceinline__ float rbf(float f) { return bf2f(f2bf(f)); }

__global__ void k_ln(const u16* x, u16* xn) {
  int row = blockIdx.x; int t = threadIdx.x;
  float v[8]; float s = 0;
  for (int i = 0; i < 8; i++) { v[i] = bf2f(x[row * 512 + t * 8 + i]); s += v[i]; }
  for (int o = 32; o; o >>= 1) s += __shfl_xor(s, o);
  float mean = s / 512.f; float q = 0;
  for (int i = 0; i < 8; i++) { float d = v[i] - mean; q += d * d; }
  for (int o = 32; o; o >>= 1) q += __shfl_xor(q, o);
  float var = rbf(q / 512.f); float m = rbf(mean);
  float sd = rbf(sqrtf(rbf(var + 1e-5f)));
  for (int i = 0; i < 8; i++) xn[row * 512 + t * 8 + i] = f2bf(rbf(v[i] - m) / sd);
}

template <int RELU>
__global__ void k_gemm(const u16* A, const u16* B, u16* C, int M, int N, int K) {
  __shared__ float As[32][33], Bs[32][33];
  int tx = threadIdx.x & 31, ty = threadIdx.x >> 5;
  int r0 = blockIdx.y * 32, c0 = blockIdx.x * 32;
  float acc[4] = {0, 0, 0, 0};
  for (int k0 = 0; k0 < K; k0 += 32) {
    for (int i = 0; i < 4; i++) {
      int r = ty + i * 8;
      As[r][tx] = (r0 + r < M) ? bf2f(A[(size_t)(r0 + r) * K + k0 + tx]) : 0.f;
      Bs[r][tx] = bf2f(B[(size_t)(k0 + r) * N + c0 + tx]);
    }
    __syncthreads();
    for (int k = 0; k < 32; k++) for (int i = 0; i < 4; i++) acc[i] += As[ty + i * 8][k] * Bs[k][tx];
    __syncthreads();
  }
  for (int i = 0; i < 4; i++) {
    int r = r0 + ty + i * 8;
    if (r < M) { float v = rbf(acc[i]); if (RELU) v = fmaxf(v, 0.f); C[(size_t)r * N + c0 + tx] = f2bf(v); }
  }
}

__global__ void k_attn(const u16* q, int qstride_rows, const u16* mk, const u16* mv, const u16* K, const u16* V,
                       u16* o, int is_mem, int seg0) {
  int h = blockIdx.y, r = blockIdx.x, t = threadIdx.x;
  int seg = is_mem ? seg0 : r / 32;
  int nk = is_mem ? 32 : (r % 32) + 1;
  const u16* mkk = mk + (size_t)seg * 8 * 4096; const u16* mvv = mv + (size_t)seg * 8 * 4096;
  __shared__ float qs[128], sc[40];
  qs[t] = bf2f(q[(size_t)r * 4096 + h * 128 + t]);
  __syncthreads();
  if (t < 40) {
    const u16* kr = t < 8 ? mkk + t * 4096 + h * 128 : K + (size_t)(seg * 32 + t - 8) * 4096 + h * 128;
    float s = 0; for (int e = 0; e < 128; e++) s += qs[e] * bf2f(kr[e]);
    s = rbf(rbf(s) * 0.08838834764831845f);
    if (t >= 8 + nk) s = rbf(-1e9f);
    sc[t] = s;
  }
  __syncthreads();
  float mx = -3e38f; for (int j = 0; j < 40; j++) mx = fmaxf(mx, sc[j]);
  float sum = 0; for (int j = 0; j < 40; j++) sum += __expf(sc[j] - mx);
  float acc = 0;
  for (int j = 0; j < 40; j++) {
    float p = rbf(__expf(sc[j] - mx) / sum);
    const u16* vr = j < 8 ? mvv + j * 4096 + h * 128 : V + (size_t)(seg * 32 + j - 8) * 4096 + h * 128;
    acc += p * bf2f(vr[t]);
  }
  o[(size_t)r * 4096 + h * 128 + t] = f2bf(acc);
}

__global__ void k_final(const u16* x, const u16* a, const u16* b, u16* out) {
  int i = blockIdx.x * 256 + threadIdx.x;
  out[i] = f2bf(rbf(bf2f(x[i]) + bf2f(a[i])) + bf2f(b[i]));
}

extern "C" void kernel_launch(void* const* d_in, const int* in_sizes, int n_in, void* d_out, int out_size, void* d_ws,
                              size_t ws_size, hipStream_t stream) {
  const u16* x = (const u16*)d_in[0]; const u16* mem0 = (const u16*)d_in[1];
  const u16* Wq = (const u16*)d_in[2]; const u16* Wk = (const u16*)d_in[3]; const u16* Wv = (const u16*)d_in[4];
  const u16* Wo = (const u16*)d_in[5]; const u16* w1 = (const u16*)d_in[8]; const u16* w2 = (const u16*)d_in[9];
  u16* p = (u16*)d_ws;
  u16* xn = p; p += 2048 * 512;
  u16* Q = p; p += 2048 * 4096; u16* K = p; p += 2048 * 4096; u16* V = p; p += 2048 * 4096; u16* O = p; p += 2048 * 4096;
  u16* mq = p; p += 8 * 4096; u16* mk = p; p += 64 * 8 * 4096; u16* mv = p; p += 64 * 8 * 4096;
  u16* mem = p; p += 8 * 512; u16* om = p; p += 8 * 4096; u16* H = p; p += 2048 * 1376;
  u16* Ao = p; p += 2048 * 512; u16* Bo = p; p += 2048 * 512;
  k_ln<<<2048, 64, 0, stream>>>(x, xn);
  k_gemm<0><<<dim3(128, 64), 256, 0, stream>>>(xn, Wq, Q, 2048, 4096, 512);
  k_gemm<0><<<dim3(128, 64), 256, 0, stream>>>(xn, Wk, K, 2048, 4096, 512);
  k_gemm<0><<<dim3(128, 64), 256, 0, stream>>>(xn, Wv, V, 2048, 4096, 512);
  k_gemm<1><<<dim3(43, 64), 256, 0, stream>>>(xn, w1, H, 2048, 1376, 512);
  k_gemm<0><<<dim3(16, 64), 256, 0, stream>>>(H, w2, Bo, 2048, 512, 1376);
  for (int s = 0; s < 64; s++) {
    const u16* m = s == 0 ? mem0 : mem;
    k_gemm<0><<<dim3(128, 1), 256, 0, stream>>>(m, Wk, mk + (size_t)s * 8 * 4096, 8, 4096, 512);
    k_gemm<0><<<dim3(128, 1), 256, 0, stream>>>(m, Wv, mv + (size_t)s * 8 * 4096, 8, 4096, 512);
    if (s < 63) {
      k_gemm<0><<<dim3(128, 1), 256, 0, stream>>>(m, Wq, mq, 8, 4096, 512);
      k_attn<<<dim3(8, 32), 128, 0, stream>>>(mq, 0, mk, mv, K, V, om, 1, s);
      k_gemm<0><<<dim3(16, 1), 256, 0, stream>>>(om, Wo, mem, 8, 512, 4096);
    }
  }
  k_attn<<<dim3(2048, 32), 128, 0, stream>>>(Q, 0, mk, mv, K, V, O, 0, 0);
  k_gemm<0><<<dim3(16, 64), 256, 0, stream>>>(O, Wo, Ao, 2048, 512, 4096);
  k_final<<<2048 * 512 / 256, 256, 0, stream>>>(x, Ao, Bo, (u16*)d_out);
}
```

```cpp
#include <hip/hip_runtime.h>
#include <stdint.h>

typedef unsigned short u16;
typedef unsigned long long u64;
typedef unsigned v4u __attribute__((ext_vector_type(4)));
typedef unsigned v2u __attribute__((ext_vector_type(2)));
typedef unsigned v8u __attribute__((ext_vector_type(8)));
typedef __bf16 bf2v __attribute__((ext_vector_type(2)));
typedef __bf16 bf8v __attribute__((ext_vector_type(8)));
typedef __bf16 bf16v __attribute__((ext_vector_type(16)));
typedef short s4v __attribute__((ext_vector_type(4)));
typedef float f4 __attribute__((ext_vector_type(4)));
typedef float f2 __attribute__((ext_vector_type(2)));
typedef unsigned v16u __attribute__((ext_vector_type(16)));
typedef unsigned v32u __attribute__((ext_vector_type(32)));
typedef __amdgpu_buffer_rsrc_t rsrc_t;
#define LDSP(T, off) ((__attribute__((address_space(3))) T*)(uintptr_t)(off))
#define DEVI __device__ __forceinline__

constexpr unsigned MBy = 1u << 20;
constexpr unsigned O_FLAG = 1792u * 1024u;
constexpr unsigned F_NONCE = O_FLAG, F_SEL = O_FLAG + 128, F_LN = O_FLAG + 1024, F_INIT = O_FLAG + 2048,
                   F_START = O_FLAG + 4096, F_H = O_FLAG + 8192, F_FOL = O_FLAG + 8192 + 256,
                   F_PROJ = O_FLAG + 16384, F_CH = O_FLAG + 32768, F_RDY = O_FLAG + 49152;
constexpr unsigned O_DBG = O_FLAG + 65536;
constexpr unsigned O_XN = 2 * MBy, O_KX = 4 * MBy, O_VX = 20 * MBy, O_QX = 36 * MBy, O_HX = 52 * MBy, O_KM = 58 * MBy,
                   O_PA = 74 * MBy,
                   O_FM = 108 * MBy, WS_BYTES = 110 * MBy;
constexpr unsigned OOB = 0x7fffffffu, SENT = 0xFFFFFFFFu;
constexpr int SC1 = 16;
constexpr int SPIN = 200000;
constexpr int FS = 5;
constexpr int PD = 2;

struct Params {
  const u16 *x, *mem0, *Wq, *Wk, *Wv, *Wo, *w1, *w2;
  u16* out;
  unsigned char* ws;
};

DEVI rsrc_t mkrs(const void* p, unsigned bytes) { return __builtin_amdgcn_make_buffer_rsrc((void*)p, (short)0, bytes, 0x00020000); }
DEVI unsigned rfl(unsigned v) { return __builtin_amdgcn_readfirstlane(v); }
DEVI unsigned pk(float a, float b) { bf2v r = {(__bf16)a, (__bf16)b}; return __builtin_bit_cast(unsigned, r); }
DEVI float blo(unsigned v) { return __uint_as_float(v << 16); }
DEVI float bhi(unsigned v) { return __uint_as_float(v & 0xffff0000u); }
DEVI float rbf(float f) { return blo(pk(f, 0.f)); }
DEVI void dma(rsrc_t rs, unsigned voff, unsigned soff, unsigned lds) {
  asm volatile("s_nop 4\n\ts_mov_b32 m0, %0\n\ts_nop 0\n\tbuffer_load_dwordx4 %1, %2, %3 offen lds" ::"s"(lds), "v"(voff), "s"(rs), "s"(soff) : "memory");
}
DEVI void dma_c(rsrc_t rs, unsigned voff, unsigned soff, unsigned lds) {
  asm volatile("s_nop 4\n\ts_mov_b32 m0, %0\n\ts_nop 0\n\tbuffer_load_dwordx4 %1, %2, %3 offen sc1 lds" ::"s"(lds), "v"(voff), "s"(rs), "s"(soff) : "memory");
}
DEVI void dma_q(rsrc_t rs, unsigned voff, unsigned soff, unsigned lds) {
  asm volatile("s_mov_b32 m0, %0\n\ts_nop 0\n\tbuffer_load_dwordx4 %1, %2, %3 offen sc1 lds" ::"s"(lds), "v"(voff), "s"(rs), "s"(soff) : "memory");
}
DEVI void dma_p(rsrc_t rs, unsigned voff, unsigned soff, unsigned lds) {
  asm volatile("s_mov_b32 m0, %0\n\ts_nop 0\n\tbuffer_load_dwordx4 %1, %2, %3 offen lds" ::"s"(lds), "v"(voff), "s"(rs), "s"(soff) : "memory");
}
template <int N> DEVI void vmw() { asm volatile("s_waitcnt vmcnt(%0)" ::"i"(N) : "memory"); }
DEVI void BAR() { asm volatile("s_waitcnt lgkmcnt(0)\n\ts_barrier" ::: "memory"); }
DEVI v4u ld4(rsrc_t rs, unsigned v, unsigned s) { return __builtin_amdgcn_raw_buffer_load_b128(rs, v, s, SC1); }
DEVI void st4(v4u d, rsrc_t rs, unsigned v, unsigned s) { __builtin_amdgcn_raw_buffer_store_b128(d, rs, v, s, SC1); }
DEVI void st_flag(rsrc_t rs, unsigned off, u64 val) {
  v2u d = {(unsigned)val, (unsigned)(val >> 32)};
  __builtin_amdgcn_raw_buffer_store_b64(d, rs, (threadIdx.x & 63) == 0 ? off : OOB, 0, SC1);
}
DEVI v2u trd(unsigned a) { return __builtin_bit_cast(v2u, __builtin_amdgcn_ds_read_tr16_b64_v4i16(LDSP(s4v, a))); }
DEVI f4 mf32(v4u a, v4u b, f4 c) { return __builtin_amdgcn_mfma_f32_16x16x32_bf16(__builtin_bit_cast(bf8v, a), __builtin_bit_cast(bf8v, b), c, 0, 0, 0); }
DEVI f4 mf16(v2u a, v2u b, f4 c) { return __builtin_amdgcn_mfma_f32_16x16x16bf16_1k(__builtin_bit_cast(s4v, a), __builtin_bit_cast(s4v, b), c, 0, 0, 0); }
DEVI f4 smf(v4u a, v8u b, f4 c, int idx) { return __builtin_amdgcn_smfmac_f32_16x16x64_bf16(__builtin_bit_cast(bf8v, a), __builtin_bit_cast(bf16v, b), c, idx, 0, 0); }
DEVI v2u pk4(f4 a) { return v2u{pk(a[0], a[1]), pk(a[2], a[3])}; }
DEVI v4u pk8(f4 a, f4 b) { return v4u{pk(a[0], a[1]), pk(a[2], a[3]), pk(b[0], b[1]), pk(b[2], b[3])}; }
DEVI unsigned mx3(unsigned a, unsigned b, unsigned c) { return max(max(a, b), c); }
DEVI unsigned mx4(v4u v, unsigned m) { return mx3(mx3(v.x, v.y, m), v.z, v.w); }
DEVI float ror8(float x) { return __builtin_bit_cast(float, __builtin_amdgcn_mov_dpp(__builtin_bit_cast(int, x), 0x128, 0xf, 0xf, true)); }
DEVI float x16max(float x) { auto s = __builtin_amdgcn_permlane16_swap(__float_as_uint(x), __float_as_uint(x), false, false); return fmaxf(__uint_as_float(s[0]), __uint_as_float(s[1])); }
DEVI float x32max(float x) { auto s = __builtin_amdgcn_permlane32_swap(__float_as_uint(x), __float_as_uint(x), false, false); return fmaxf(__uint_as_float(s[0]), __uint_as_float(s[1])); }
DEVI float x16add(float x) { auto s = __builtin_amdgcn_permlane16_swap(__float_as_uint(x), __float_as_uint(x), false, false); return __uint_as_float(s[0]) + __uint_as_float(s[1]); }
DEVI float x32add(float x) { auto s = __builtin_amdgcn_permlane32_swap(__float_as_uint(x), __float_as_uint(x), false, false); return __uint_as_float(s[0]) + __uint_as_float(s[1]); }
DEVI float hsum(float a, float b) { auto s = __builtin_amdgcn_permlane32_swap(__float_as_uint(a), __float_as_uint(b), false, false); return __uint_as_float(s[0]) + __uint_as_float(s[1]); }
template <int N> DEVI void dly() {
#pragma unroll
  for (int i = 0; i < N; i++) asm volatile("s_nop 7" ::: "memory");
}

DEVI unsigned wait_flags(rsrc_t rs, unsigned voff, u64 nonce, unsigned need, int& budget) {
  unsigned d = 0;
  while (true) {
    asm volatile("" ::: "memory");
    v2u f = __builtin_amdgcn_raw_buffer_load_b64(rs, voff, 0, SC1);
    u64 df = (((u64)f.y << 32) | f.x) - nonce;
    d = voff == OOB ? 255u : (df <= 100ull ? (unsigned)df : 0u);
    if (__all(d >= need)) break;
    if (--budget < 0) return 255u;
    __builtin_amdgcn_s_sleep(2);
  }
  for (int o = 32; o; o >>= 1) d = min(d, (unsigned)__shfl_xor((int)d, o));
  return rfl(d);
}

DEVI void ln_load(const u16* x, int seg, int lane, int w, v4u (&v)[8]) {
  rsrc_t rx = mkrs(x, 2048 * 512 * 2);
  unsigned vo = (seg * 32 + (w & 1) * 16 + (lane & 15)) * 1024 + (w >> 1) * 512 + (lane >> 4) * 16;
#pragma unroll
  for (int i = 0; i < 8; i++) v[i] = __builtin_amdgcn_raw_buffer_load_b128(rx, vo + i * 64, 0, 0);
}
DEVI void ln_apply(v4u (&v)[8], unsigned scr, int lane, int w) {
  float s = 0.f;
#pragma unroll
  for (int i = 0; i < 8; i++)
#pragma unroll
    for (int j = 0; j < 4; j++) s += blo(v[i][j]) + bhi(v[i][j]);
  s = x32add(x16add(s));
  *LDSP(float, scr + (w * 64 + lane) * 4) = s;
  BAR();
  float mean = (s + *LDSP(float, scr + ((w ^ 2) * 64 + lane) * 4)) * (1.f / 512.f);
  float q = 0.f;
#pragma unroll
  for (int i = 0; i < 8; i++)
#pragma unroll
    for (int j = 0; j < 4; j++) { float a = blo(v[i][j]) - mean, b = bhi(v[i][j]) - mean; q = fmaf(a, a, q); q = fmaf(b, b, q); }
  q = x32add(x16add(q));
  *LDSP(float, scr + 1024 + (w * 64 + lane) * 4) = q;
  BAR();
  float var = rbf((q + *LDSP(float, scr + 1024 + ((w ^ 2) * 64 + lane) * 4)) * (1.f / 512.f));
  float m = rbf(mean);
  float inv = 1.f / rbf(sqrtf(rbf(var + 1e-5f)));
#pragma unroll
  for (int i = 0; i < 8; i++)
#pragma unroll
    for (int j = 0; j < 4; j++) {
      unsigned d = pk(blo(v[i][j]) - m, bhi(v[i][j]) - m);
      v[i][j] = pk(blo(d) * inv, bhi(d) * inv);
    }
}
DEVI void role_ln(const Params& p, rsrc_t rs, u64 nonce, int seg, int lane, int w) {
  v4u v[8];
  ln_load(p.x, seg, lane, w, v);
  ln_apply(v, 0, lane, w);
#pragma unroll
  for (int i = 0; i < 8; i++) st4(v[i], rs, lane * 16, O_XN + seg * 32768 + (((w >> 1) * 8 + i) * 2 + (w & 1)) * 1024);
  vmw<0>();
  BAR();
  if (w == 0) st_flag(rs, F_LN + seg * 8, nonce + 1);
}

template <bool VTYPE, bool WIDE>
DEVI void role_proj(rsrc_t rs, rsrc_t rw, unsigned rowbytes, unsigned colbyte, unsigned obase, unsigned ostride, unsigned fl,
                    bool relu, bool full, int s0, int s1, u64 nonce, int lane, int w, const u16* own) {
  int budget = SPIN;
  v4u xa[8], xc[8];
  if (own) { ln_load(own, 0, lane, w, xa); ln_load(own, 1, lane, w, xc); }
  auto fetch = [&](unsigned cbyte) {
    const int r = lane >> 4, ch = lane & 15;
    int wv = w;
    if (WIDE) asm volatile("" : "+s"(wv));
#pragma unroll
    for (int q = 0; q < 32; q++) {
      int rho = r | (((q >> 1) & 1) << 2);
      unsigned vo = r * rowbytes + cbyte + (((ch >> 1) ^ rho) * 32) + (ch & 1) * 16;
      dma(rw, vo, (wv * 128 + q * 4) * rowbytes, (wv * 128 + q * 4) * 256);
    }
  };
  if (WIDE && s0 && w == 0) st_flag(rs, fl, nonce + s0);
  fetch(colbyte);
  const unsigned xo = ((w >> 1) * 16 + (w & 1)) * 1024 + lane * 16;
  if (own) {
    ln_apply(xa, 131072, lane, w);
    ln_apply(xc, 131072 + 2048, lane, w);
    BAR();
#pragma unroll
    for (int i = 0; i < 8; i++) *LDSP(v4u, 131072 + xo + i * 2048) = xa[i];
    vmw<0>();
  } else {
    wait_flags(rs, lane <= PD ? F_LN + (s0 + lane) * 8 : OOB, nonce, 1, budget);
#pragma unroll
    for (int q = 0; q < 8; q++) dma_c(rs, lane * 16, O_XN + s0 * 32768 + (w * 8 + q) * 1024, 131072 + (w * 8 + q) * 1024);
    vmw<8>();
  }
  BAR();
  constexpr int NT = VTYPE ? 2 : 4, NX = VTYPE || WIDE ? 2 : 1;
  v4u W[NT][16];
#pragma unroll 1
  for (int ps = 0; ps < (WIDE ? 2 : 1); ps++) {
    if (ps) { BAR(); fetch(colbyte + 256); vmw<0>(); BAR(); }
    if (WIDE && (w >> 1) != ps) continue;
    const int g = lane >> 4, r = (lane >> 2) & 3, cq = lane & 3;
    const int rho = r | ((g & 1) << 2), u0 = VTYPE ? 2 * w : WIDE ? 4 * (w & 1) : 4 * (w >> 1);
#pragma unroll
    for (int T = 0; T < NT; T++) {
      unsigned a = (8 * g + r) * 256 + (((u0 + T) ^ rho) * 32) + cq * 8;
#pragma unroll
      for (int k = 0; k < 16; k++) {
        v2u lo = trd(a + k * 8192), hi = trd(a + k * 8192 + 1024);
        W[T][k] = v4u{lo.x, lo.y, hi.x, hi.y};
      }
    }
  }
  BAR();
  if (own) {
#pragma unroll
    for (int i = 0; i < 8; i++) *LDSP(v4u, xo + i * 2048) = xc[i];
  } else {
#pragma unroll
    for (int q = 0; q < 8; q++) dma_c(rs, lane * 16, O_XN + (s0 + 1) * 32768 + (w * 8 + q) * 1024, (w * 8 + q) * 1024);
  }
  unsigned cur = 4, nxt = PD - 1;
  const unsigned vo = lane * 16, vo2 = full || w < 2 ? lane * 16 : OOB;
  const unsigned ow = obase + (VTYPE ? w * 2048 : WIDE ? w * 4096 : (w >> 1) * 4096 + (w & 1) * 1024);
  for (int s = s0; s < s1; s++) {
    if (s == s0 || s == s0 + 2) vmw<8>(); else vmw<WIDE ? 12 : 10>();
    const unsigned dv = own && s < 2 ? OOB : lane * 16;
    BAR();
    const unsigned xb = cur * 32768 + lane * 16 + (VTYPE || WIDE ? 0 : (w & 1) * 1024);
    v4u x[4][NX];
#pragma unroll
    for (int k = 0; k < 3; k++)
#pragma unroll
      for (int i = 0; i < NX; i++) x[k][i] = *LDSP(v4u, xb + k * 2048 + i * 1024);
    if (s >= s0 + PD + 2 && w == 0) st_flag(rs, fl, nonce + s + 1 - PD);
    const unsigned gsrc = O_XN + min(s + PD, s1 - 1) * 32768 + w * 8192, ldst = nxt * 32768 + w * 8192;
    f4 acc[4] = {}, ac2[4] = {};
#pragma unroll
    for (int k = 0; k < 16; k++) {
      if (k + 3 < 16) {
#pragma unroll
        for (int i = 0; i < NX; i++) x[(k + 3) & 3][i] = *LDSP(v4u, xb + (k + 3) * 2048 + i * 1024);
      }
      __builtin_amdgcn_sched_barrier(0);
      const v4u x0 = x[k & 3][0], x1 = x[k & 3][NX - 1];
      if (VTYPE) {
        acc[0] = mf32(x0, W[0][k], acc[0]); acc[2] = mf32(x0, W[1][k], acc[2]);
        if (k & 1) dma_q(rs, dv, gsrc + (k >> 1) * 1024, ldst + (k >> 1) * 1024);
        acc[1] = mf32(x1, W[0][k], acc[1]); acc[3] = mf32(x1, W[1][k], acc[3]);
      } else {
        acc[0] = mf32(W[0][k], x0, acc[0]); acc[1] = mf32(W[1][k], x0, acc[1]);
        if (k & 1) dma_q(rs, dv, gsrc + (k >> 1) * 1024, ldst + (k >> 1) * 1024);
        acc[2] = mf32(W[NT - 2][k], x0, acc[2]); acc[3] = mf32(W[NT - 1][k], x0, acc[3]);
        if (WIDE) {
#pragma unroll
          for (int a = 0; a < 4; a++) ac2[a] = mf32(W[a % NT][k], x1, ac2[a]);
        }
      }
      __builtin_amdgcn_sched_barrier(0);
    }
    if (relu) {
#pragma unroll
      for (int a = 0; a < 4; a++)
#pragma unroll
        for (int i = 0; i < 4; i++) { float v = acc[a][i]; acc[a][i] = v < 0.f ? 0.f : v; }
    }
    st4(pk8(acc[0], acc[1]), rs, vo, ow + s * ostride);
    st4(pk8(acc[2], acc[3]), rs, vo2, ow + s * ostride + (VTYPE ? 1024 : 2048));
    if (WIDE) {
      st4(pk8(ac2[0], ac2[1]), rs, vo, ow + s * ostride + 1024);
      st4(pk8(ac2[2], ac2[3]), rs, vo, ow + s * ostride + 3072);
    }
    if (s == s0 + (own ? 1 : 0)) wait_flags(rs, F_LN + lane * 8, nonce, 1, budget);
    if (s == s0 + 1 || (s == 0 && !relu && !own)) {
      if (own) {
#pragma unroll
        for (int q = 0; q < 16; q++) dma_c(rs, lane * 16, O_XN + (2 + (q >> 3)) * 32768 + (w * 8 + (q & 7)) * 1024, (1 + (q >> 3)) * 32768 + (w * 8 + (q & 7)) * 1024);
        vmw<16>();
      } else vmw<0>();
      BAR();
      if (w == 0) st_flag(rs, fl, nonce + s + 1);
    }
    cur = cur == 4 ? 0 : cur + 1;
    nxt = nxt == 4 ? 0 : nxt + 1;
  }
  vmw<0>();
  BAR();
  if (w == 0) st_flag(rs, fl, nonce + (WIDE ? 64 : s1));
}

DEVI void role_follow(const Params& p, rsrc_t rs, u64 nonce, int f, int set, int lane, int w) {
  int budget = SPIN;
  const int hp = f >> 1, ch = f & 1, g = lane >> 4, c = lane & 15;
  wait_flags(rs, F_INIT + lane * 8, nonce, 1, budget);
  rsrc_t rw = mkrs(p.Wo, 4096 * 512 * 2);
  {
    const int r = lane >> 5, cc = lane & 31;
#pragma unroll
    for (int q = 0; q < 32; q++) {
      int rho = (2 * q + r) & 7;
      unsigned vo = r * 1024 + ch * 512 + (((cc >> 1) ^ rho) * 32) + (cc & 1) * 16;
      dma(rw, vo, (hp * 256 + w * 64 + q * 2) * 1024, (w * 64 + q * 2) * 512);
    }
  }
  vmw<0>();
  BAR();
  v4u W[8][4];
  {
    const int r = (lane >> 2) & 3, cq = lane & 3;
    const int rho = (4 * g + r) & 7;
#pragma unroll
    for (int T = 0; T < 4; T++) {
      unsigned a = (4 * g + r) * 512 + (((4 * w + T) ^ rho) * 32) + cq * 8;
#pragma unroll
      for (int k = 0; k < 8; k++) {
        v2u lo = trd(a + k * 16384), hi = trd(a + k * 16384 + 8192);
        W[k][T] = v4u{lo.x, lo.y, hi.x, hi.y};
      }
    }
  }
  BAR();
  const int hi_ = w >> 1, tq = w & 1, h = 2 * hp + hi_;
  const unsigned fx = lane < 3 ? F_PROJ + (lane * 32 + h) * 128 : lane == 3 ? F_PROJ + (64 + (h ^ 1)) * 128 : OOB;
  const unsigned fm = lane < 2 ? F_CH + (2 * h + lane) * 128 : OOB;
  const float kap = 0.12751743f;
  unsigned knownx = 0, knownm = 0;
  const unsigned l16 = lane * 16;
  auto src = [&](int s, int q) { const int e = tq * 12 + q; return (e < 8 ? O_QX : e < 16 ? O_KX : O_VX) + (s * 32 + h) * 8192 + (e & 7) * 1024; };
  auto after = [&](int s) { return s < FS - 1 ? s + 1 : s + 3; };
  const int first = set == 2 ? 0 : FS + set;
  const unsigned fl = F_FOL + set * 256 + (ch * 16 + hp) * 8;
  knownx = wait_flags(rs, fx, nonce, after(first) + 1, budget);
#pragma unroll
  for (int i = 0; i < 2; i++)
#pragma unroll
    for (int q = 0; q < 12; q++) dma_c(rs, l16, src(i ? after(first) : first, q), i * 49152 + hi_ * 24576 + (tq * 12 + q) * 1024);
  vmw<0>();
  BAR();
  const float NEG = -1.12938353e10f;
  int par = 0, prev = -1;
  for (int s = first; s < 64; prev = s, s = after(s), par ^= 1) {
    const unsigned sb = (s * 32 + h) * 8192, xb = par * 49152 + hi_ * 24576 + l16;
    if (knownm < (unsigned)s + 1) knownm = wait_flags(rs, fm, nonce, s + 1, budget);
    v4u Km[4], Vm[4], Qf[4], Vf[8];
#pragma unroll
    for (int u = 0; u < 4; u++) Km[u] = ld4(rs, (g * 8 + (c & 7)) * 16, O_KM + sb + u * 512);
#pragma unroll
    for (int u = 0; u < 4; u++) Vm[u] = ld4(rs, l16, O_KM + sb + 4096 + u * 1024);
    asm volatile("" ::: "memory");
    f4 s0 = {}, s1 = {}, sm = {};
#pragma unroll
    for (int u = 0; u < 4; u++) {
      Qf[u] = *LDSP(v4u, xb + u * 2048 + tq * 1024);
      s0 = mf32(*LDSP(v4u, xb + 8192 + u * 2048), Qf[u], s0);
      s1 = mf32(*LDSP(v4u, xb + 8192 + u * 2048 + 1024), Qf[u], s1);
    }
#pragma unroll
    for (int u = 0; u < 8; u++) Vf[u] = *LDSP(v4u, xb + 16384 + u * 1024);
    float mx = NEG;
#pragma unroll
    for (int i = 0; i < 4; i++) {
      bool dm = 4 * g + i > c;
      if (tq ? false : dm) s0[i] = NEG;
      if (tq ? dm : true) s1[i] = NEG;
      mx = fmaxf(mx, fmaxf(s0[i], s1[i]));
    }
    asm volatile("" : "+v"(mx)::"memory");
#pragma unroll
    for (int u = 0; u < 4; u++) sm = mf32(Km[u], Qf[u], sm);
    mx = fmaxf(fmaxf(mx, fmaxf(sm[0], sm[1])), fmaxf(sm[2], sm[3]));
    mx = x32max(x16max(mx));
    asm volatile("" :: "v"(mx));
    float mk = -mx * kap, sum = 0.f, summ = 0.f;
#pragma unroll
    for (int i = 0; i < 4; i++) {
      s0[i] = __builtin_amdgcn_exp2f(fmaf(s0[i], kap, mk));
      s1[i] = __builtin_amdgcn_exp2f(fmaf(s1[i], kap, mk));
      sm[i] = __builtin_amdgcn_exp2f(fmaf(sm[i], kap, mk));
      sum += s0[i] + s1[i];
      summ += sm[i];
    }
    sum = x32add(x16add(fmaf(summ, 0.5f, sum)));
    float ri = __builtin_amdgcn_rcpf(sum);
    v4u P = pk8(s0 * ri, s1 * ri);
    v2u Pm = pk4(sm * ri);
    const unsigned ob = 98304 + par * 16384 + l16;
    f4 o[8];
#pragma unroll
    for (int u = 0; u < 8; u++) o[u] = mf32(Vf[u], P, f4{});
#pragma unroll
    for (int u = 0; u < 4; u++) {
      o[2 * u] = mf16(v2u{Vm[u].x, Vm[u].y}, Pm, o[2 * u]);
      o[2 * u + 1] = mf16(v2u{Vm[u].z, Vm[u].w}, Pm, o[2 * u + 1]);
    }
#pragma unroll
    for (int u = 0; u < 4; u++) *LDSP(v4u, ob + ((hi_ * 4 + u) * 2 + tq) * 1024) = pk8(o[2 * u], o[2 * u + 1]);
    vmw<0>();
    BAR();
    v4u b[3][2];
#pragma unroll
    for (int k = 0; k < 2; k++) { b[k][0] = *LDSP(v4u, ob + k * 2048); b[k][1] = *LDSP(v4u, ob + k * 2048 + 1024); }
    if (w == 0 && prev >= 0) st_flag(rs, fl, nonce + prev + 1);
    const int s2 = min(after(after(s)), 63);
    if (knownx < (unsigned)s2 + 1) knownx = wait_flags(rs, fx, nonce, s2 + 1, budget);
    const unsigned ld = par * 49152 + hi_ * 24576 + tq * 12288;
    f4 acc[4][2];
#pragma unroll
    for (int k = 0; k < 8; k++) {
      if (k + 2 < 8) { b[(k + 2) % 3][0] = *LDSP(v4u, ob + (k + 2) * 2048); b[(k + 2) % 3][1] = *LDSP(v4u, ob + (k + 2) * 2048 + 1024); }
      const int q0 = k < 4 ? 2 * k : 4 + k;
      if (k == 0) {
        asm volatile(
            "v_mfma_f32_16x16x32_bf16 %0, %8, %12, 0\n\tv_mfma_f32_16x16x32_bf16 %1, %8, %13, 0\n\ts_mov_b32 m0, %14\n\t"
            "v_mfma_f32_16x16x32_bf16 %2, %9, %12, 0\n\tbuffer_load_dwordx4 %15, %16, %17 offen sc1 lds\n\t"
            "v_mfma_f32_16x16x32_bf16 %3, %9, %13, 0\n\tv_mfma_f32_16x16x32_bf16 %4, %10, %12, 0\n\t"
            "v_mfma_f32_16x16x32_bf16 %5, %10, %13, 0\n\tv_mfma_f32_16x16x32_bf16 %6, %11, %12, 0\n\t"
            "buffer_load_dwordx4 %15, %16, %17 offen offset:1024 sc1 lds\n\tv_mfma_f32_16x16x32_bf16 %7, %11, %13, 0"
            : "=&v"(acc[0][0]), "=&v"(acc[0][1]), "=&v"(acc[1][0]), "=&v"(acc[1][1]), "=&v"(acc[2][0]), "=&v"(acc[2][1]), "=&v"(acc[3][0]), "=&v"(acc[3][1])
            : "v"(W[k][0]), "v"(W[k][1]), "v"(W[k][2]), "v"(W[k][3]), "v"(b[k % 3][0]), "v"(b[k % 3][1]), "s"(ld + q0 * 1024), "v"(l16), "s"(rs), "s"(src(s2, q0))
            : "memory");
      } else if (k < 4) {
        asm volatile(
            "v_mfma_f32_16x16x32_bf16 %0, %8, %12, %0\n\tv_mfma_f32_16x16x32_bf16 %1, %8, %13, %1\n\ts_mov_b32 m0, %14\n\t"
            "v_mfma_f32_16x16x32_bf16 %2, %9, %12, %2\n\tbuffer_load_dwordx4 %15, %16, %17 offen sc1 lds\n\t"
            "v_mfma_f32_16x16x32_bf16 %3, %9, %13, %3\n\tv_mfma_f32_16x16x32_bf16 %4, %10, %12, %4\n\t"
            "v_mfma_f32_16x16x32_bf16 %5, %10, %13, %5\n\tv_mfma_f32_16x16x32_bf16 %6, %11, %12, %6\n\t"
            "buffer_load_dwordx4 %15, %16, %17 offen offset:1024 sc1 lds\n\tv_mfma_f32_16x16x32_bf16 %7, %11, %13, %7"
            : "+v"(acc[0][0]), "+v"(acc[0][1]), "+v"(acc[1][0]), "+v"(acc[1][1]), "+v"(acc[2][0]), "+v"(acc[2][1]), "+v"(acc[3][0]), "+v"(acc[3][1])
            : "v"(W[k][0]), "v"(W[k][1]), "v"(W[k][2]), "v"(W[k][3]), "v"(b[k % 3][0]), "v"(b[k % 3][1]), "s"(ld + q0 * 1024), "v"(l16), "s"(rs), "s"(src(s2, q0))
            : "memory");
      } else {
        asm volatile(
            "v_mfma_f32_16x16x32_bf16 %0, %8, %12, %0\n\tv_mfma_f32_16x16x32_bf16 %1, %8, %13, %1\n\ts_mov_b32 m0, %14\n\t"
            "v_mfma_f32_16x16x32_bf16 %2, %9, %12, %2\n\tbuffer_load_dwordx4 %15, %16, %17 offen sc1 lds\n\t"
            "v_mfma_f32_16x16x32_bf16 %3, %9, %13, %3\n\tv_mfma_f32_16x16x32_bf16 %4, %10, %12, %4\n\t"
            "v_mfma_f32_16x16x32_bf16 %5, %10, %13, %5\n\tv_mfma_f32_16x16x32_bf16 %6, %11, %12, %6\n\t"
            "v_mfma_f32_16x16x32_bf16 %7, %11, %13, %7"
            : "+v"(acc[0][0]), "+v"(acc[0][1]), "+v"(acc[1][0]), "+v"(acc[1][1]), "+v"(acc[2][0]), "+v"(acc[2][1]), "+v"(acc[3][0]), "+v"(acc[3][1])
            : "v"(W[k][0]), "v"(W[k][1]), "v"(W[k][2]), "v"(W[k][3]), "v"(b[k % 3][0]), "v"(b[k % 3][1]), "s"(ld + q0 * 1024), "v"(l16), "s"(rs), "s"(src(s2, q0))
            : "memory");
      }
    }
    asm volatile("s_nop 7"
                 : "+v"(acc[0][0]), "+v"(acc[0][1]), "+v"(acc[1][0]), "+v"(acc[1][1]), "+v"(acc[2][0]), "+v"(acc[2][1]), "+v"(acc[3][0]), "+v"(acc[3][1]));
#pragma unroll
    for (int T = 0; T < 4; T++)
      st4(pk8(acc[T][0], acc[T][1]), rs, l16, O_PA + ((s * 32 + ch * 16 + w * 4 + T) * 16 + hp) * 1024);
  }
  vmw<0>();
  BAR();
  if (w == 0) st_flag(rs, fl, nonce + 64);
}

DEVI void role_final(const Params& p, rsrc_t rs, u64 nonce, int j, int lane, int w) {
  int budget = SPIN;
  const int g = lane >> 4, c = lane & 15, ct = (j & 7) * 4 + w, sg = j >> 3;
  const unsigned l16 = lane * 16;
  wait_flags(rs, F_INIT + lane * 8, nonce, 1, budget);
  rsrc_t rw = mkrs(p.w2, 1376 * 512 * 2);
  v4u W[43];
  {
    const int r = (lane >> 2) & 3, cq = lane & 3;
    const unsigned a = w * 22528 + (4 * g + r) * 32 + cq * 8;
#pragma unroll
    for (int hf = 0; hf < 2; hf++) {
#pragma unroll
      for (int q = 0; q < 22 - hf; q++) dma(rw, (lane >> 1) * 1024 + ct * 32 + (lane & 1) * 16, (hf * 22 + q) * 32768, w * 22528 + q * 1024);
      vmw<0>();
#pragma unroll
      for (int k = 0; k < 22 - hf; k++) {
        v2u lo = trd(a + k * 1024), hi = trd(a + k * 1024 + 512);
        W[hf * 22 + k] = v4u{lo.x, lo.y, hi.x, hi.y};
      }
      asm volatile("s_waitcnt lgkmcnt(0)" ::: "memory");
    }
  }
  rsrc_t rx = mkrs(p.x, 2048 * 512 * 2), ro = mkrs(p.out, 2048 * 512 * 2);
  const unsigned vout = c * 1024 + ct * 32 + g * 8;
  {
    unsigned knownh = 0;
    auto issue = [&](int cn, unsigned slot) {
      const int s = sg + 4 * min(cn >> 2, 15);
      if (knownh < (unsigned)s + 1) knownh = wait_flags(rs, lane < 11 ? F_H + (lane + (s >> 5) * 11) * 8 : OOB, nonce, s + 1, budget);
      const unsigned src = O_HX + s * 90112 + (cn & 3) * 22528 + w * 6144, dst = slot * 24576 + w * 6144;
#pragma unroll
      for (int q = 0; q < 6; q++) dma_c(rs, l16, src + q * 1024, dst + q * 1024);
    };
    BAR();
    for (int cn = 0; cn < 5; cn++) issue(cn, cn);
    unsigned cur = 0;
    for (int i = 0; i < 16; i++) {
      f4 b0 = {}, b1 = {};
#pragma unroll
      for (int q = 0; q < 4; q++) {
        vmw<24>();
        BAR();
        issue(4 * i + q + 5, cur ? cur - 1 : 5);
        const unsigned xb = cur * 24576 + l16;
#pragma unroll
        for (int k = 0; k < (q < 3 ? 11 : 10); k++) {
          b0 = mf32(W[q * 11 + k], *LDSP(v4u, xb + k * 2048), b0);
          b1 = mf32(W[q * 11 + k], *LDSP(v4u, xb + k * 2048 + 1024), b1);
        }
        cur = cur == 5 ? 0 : cur + 1;
      }
      const v4u bb = pk8(b0, b1);
      __builtin_amdgcn_raw_buffer_store_b64(v2u{bb.x, bb.y}, ro, vout, (sg + 4 * i) * 32768, 0);
      __builtin_amdgcn_raw_buffer_store_b64(v2u{bb.z, bb.w}, ro, vout, (sg + 4 * i) * 32768 + 16384, 0);
    }
    vmw<0>();
  }
  const unsigned ff = lane < 16 ? F_FOL + ((ct >> 4) * 16 + lane) * 8 : OOB;
  unsigned known[3] = {0, 0, 0};
  for (int i = 0; i < 16; i++) {
    const int s = sg + 4 * i;
    const v2u x0 = __builtin_amdgcn_raw_buffer_load_b64(rx, vout, s * 32768, 0), x1 = __builtin_amdgcn_raw_buffer_load_b64(rx, vout, s * 32768 + 16384, 0);
    const v2u y0 = __builtin_amdgcn_raw_buffer_load_b64(ro, vout, s * 32768, 0), y1 = __builtin_amdgcn_raw_buffer_load_b64(ro, vout, s * 32768 + 16384, 0);
    const v4u bb = {y0.x, y0.y, y1.x, y1.y};
    const int set = s < FS ? 2 : (s - FS) % 3;
#pragma unroll
    for (int q = 0; q < 3; q++)
      if (set == q && known[q] < (unsigned)s + 1) known[q] = wait_flags(rs, ff + (lane < 16 ? q * 256 : 0), nonce, s + 1, budget);
    v4u t[16];
#pragma unroll
    for (int q = 0; q < 16; q++) t[q] = ld4(rs, l16, O_PA + ((s * 32 + ct) * 16 + q) * 1024);
    f4 a0 = {}, a1 = {};
#pragma unroll
    for (int q = 0; q < 16; q++) {
      a0 += f4{blo(t[q].x), bhi(t[q].x), blo(t[q].y), bhi(t[q].y)};
      a1 += f4{blo(t[q].z), bhi(t[q].z), blo(t[q].w), bhi(t[q].w)};
    }
    const v4u ab = pk8(a0, a1);
    const v4u xin = {x0.x, x0.y, x1.x, x1.y};
    v4u o;
#pragma unroll
    for (int i2 = 0; i2 < 4; i2++) {
      unsigned u = pk(blo(xin[i2]) + blo(ab[i2]), bhi(xin[i2]) + bhi(ab[i2]));
      o[i2] = pk(blo(u) + blo(bb[i2]), bhi(u) + bhi(bb[i2]));
    }
    __builtin_amdgcn_raw_buffer_store_b64(v2u{o.x, o.y}, ro, vout, s * 32768, 0);
    __builtin_amdgcn_raw_buffer_store_b64(v2u{o.z, o.w}, ro, vout, s * 32768 + 16384, 0);
  }
  if (j == 0 && w == 1) {
    for (int q = 0; q < 4; q++) wait_flags(rs, F_START + (q * 64 + lane) * 8, nonce, 1, budget);
    st_flag(rs, F_NONCE, nonce + 128);
  }
}

#define CHAIN_ASM \
  "s_mov_b32 s60, 0x00ff00ff\n" \
  "s_mov_b32 s61, 0x00ff00ff\n" \
  "s_mov_b32 s34, 0x3f803f80\n" \
  "s_mov_b32 s35, 0x3f803f80\n" \
  "s_mov_b32 s36, 0x3f003f00\n" \
  "s_mov_b32 s37, 0x3f003f00\n" \
  "s_mov_b32 s38, 0\n" \
  "s_mov_b32 s39, -1\n" \
  "s_mov_b32 s31, 0\n" \
  "s_waitcnt vmcnt(0)\n" \
  "ds_read_b128 v[88:91], v117 offset:16384\n" \
  "ds_read_b128 v[92:95], v117 offset:17408\n" \
  "ds_read_b128 v[96:99], v117 offset:24576\n" \
  "ds_read_b64 v[100:101], v117 offset:25608\n" \
  "ds_read_b64 v[102:103], v117 offset:25600\n" \
  "ds_read_b128 v[32:35], v121 offset:0\n" \
  "ds_read_b128 v[36:39], v121 offset:1024\n" \
  "ds_read_b128 v[40:43], v121 offset:2048\n" \
  "ds_read_b128 v[44:47], v121 offset:3072\n" \
  "ds_read_b128 v[48:51], v121 offset:4096\n" \
  "ds_read_b128 v[52:55], v121 offset:5120\n" \
  "ds_read_b128 v[56:59], v121 offset:6144\n" \
  "ds_read_b128 v[60:63], v121 offset:7168\n" \
  "v_mov_b64 v[64:65], 0\n" \
  "v_mov_b64 v[66:67], 0\n" \
  "v_mov_b64 v[68:69], 0\n" \
  "v_mov_b64 v[70:71], 0\n" \
  "v_mov_b64 v[80:81], 0\n" \
  "v_mov_b64 v[82:83], 0\n" \
  "v_mov_b64 v[84:85], 0\n" \
  "v_mov_b64 v[86:87], 0\n" \
  "v_mov_b64 v[24:25], -1\n" \
  "v_mov_b64 v[26:27], -1\n" \
  "v_mov_b32 v113, s30\n" \
  "ds_write_b128 v117, v[24:27] offset:8192\n" \
  "ds_write_b64 v117, v[24:25] offset:9216\n" \
  "ds_read_b32 v28, v117 offset:9224\n" \
  "v_mov_b64 v[72:73], 0\n" \
  "v_mov_b64 v[74:75], 0\n" \
  "v_mov_b64 v[76:77], 0\n" \
  "v_mov_b64 v[78:79], 0\n" \
  "s_waitcnt lgkmcnt(0)\n" \
  "s_memrealtime s[92:93]\n" \
  "s_waitcnt lgkmcnt(0)\n" \
  "s_mov_b32 s100, s45\n" \
  "s_sub_u32 s68, s92, 900\n" \
  "s_mov_b32 s69, s92\n" \
  "s_sub_u32 s97, s92, 50000\n" \
  "s_mov_b32 s98, 128\n" \
  "s_mov_b32 s68, s92\n" \
  "s_mov_b32 s69, s68\n" \
  "s_add_u32 s97, s68, 50\n" \
  "s_mov_b32 s98, 170\n" \
  "s_add_u32 s47, s46, 0x80000\n" \
  "s_add_u32 s95, s55, 1638400\n" \
  "s_sub_u32 s96, s56, 1867776\n" \
  "s_mov_b32 s98, 352\n" \
  "s_sub_u32 s69, s68, 50000\n" \
  "s_mov_b32 s31, 40\n" \
  "s_cmp_eq_u32 s54, 1\n" \
  "s_cselect_b32 s30, s96, 0x7fffffff\n" \
  "v_mov_b32 v113, s30\n" \
  "LOOP%=:\n" \
  "buffer_load_dwordx4 v[0:3], v117, s[40:43], s100 offen sc1\n" \
  "buffer_load_dwordx4 v[4:7], v117, s[40:43], s100 offen offset:1024 sc1\n" \
  "s_add_u32 s52, s52, 0x40000\n" \
  "s_sleep 3\n" \
  "buffer_store_dwordx4 v[108:111], v28, s[40:43], s53 offen sc1\n" \
  "buffer_store_dwordx4 v[24:27], v123, s[40:43], s47 offen sc1\n" \
  "s_add_u32 s53, s53, 0x40000\n" \
  "s_add_u32 s62, s50, s44\n" \
  "s_addc_u32 s63, s51, 0\n" \
  "s_sub_u32 s62, s62, 1\n" \
  "s_subb_u32 s63, s63, 0\n" \
  "s_cmp_eq_u32 s54, 3\n" \
  "s_cselect_b32 s64, s56, 0x7fffffff\n" \
  "v_mov_b32 v12, s62\n" \
  "v_mov_b32 v13, s63\n" \
  "v_mov_b32 v14, s64\n" \
  "s_mov_b64 exec, 1\n" \
  "buffer_store_dwordx2 v[12:13], v14, s[40:43], 0 offen sc1\n" \
  "s_mov_b64 exec, -1\n" \
  "s_cmp_lg_u32 s54, 2\n" \
  "s_cbranch_scc1 NOPA%=\n" \
  "s_add_u32 s63, s44, 6\n" \
  "s_min_u32 s63, s63, 64\n" \
  "s_cmp_ge_u32 s48, s63\n" \
  "s_cbranch_scc1 NOPA%=\n" \
  "v_lshrrev_b32 v12, 2, v117\n" \
  "v_and_b32 v13, 8, v12\n" \
  "v_and_b32 v12, 4, v12\n" \
  "v_lshl_or_b32 v12, v13, 9, v12\n" \
  "s_mov_b32 m0, 36864\n" \
  "s_mov_b64 exec, 15\n" \
  "buffer_load_dword v12, s[40:43], s57 offen sc1 lds\n" \
  "s_mov_b64 exec, -1\n" \
  "NOPA%=:\n" \
  "s_mul_i32 s63, s54, 14336\n" \
  "s_sub_u32 s99, s95, s63\n" \
  "GA%=:\n" \
  "s_waitcnt vmcnt(4)\n" \
  "v_smfmac_f32_16x16x64_bf16 v[64:67], v[0:3], v[128:135], v125\n" \
  "ds_write_b128 v117, v[0:3] offset:0\n" \
  "v_smfmac_f32_16x16x64_bf16 v[68:71], v[0:3], v[192:199], v125\n" \
  "v_max3_u32 v8, v0, v1, v2\n" \
  "s_waitcnt vmcnt(3)\n" \
  "v_max3_u32 v8, v8, v3, v4\n" \
  "v_max3_u32 v9, v5, v6, v7\n" \
  "v_smfmac_f32_16x16x64_bf16 v[64:67], v[4:7], v[136:143], v125\n" \
  "ds_write_b128 v117, v[4:7] offset:1024\n" \
  "v_smfmac_f32_16x16x64_bf16 v[68:71], v[4:7], v[200:207], v125\n" \
  "v_max_u32 v8, v8, v9\n" \
  "v_smfmac_f32_16x16x64_bf16 v[72:75], v[0:3], a[0:7], v125\n" \
  "v_cmp_eq_u32 vcc, -1, v8\n" \
  "v_smfmac_f32_16x16x64_bf16 v[76:79], v[0:3], a[64:71], v125\n" \
  "s_cbranch_vccnz RETA%=\n" \
  "GAOK%=:\n" \
  "v_smfmac_f32_16x16x64_bf16 v[72:75], v[4:7], a[8:15], v125\n" \
  "s_waitcnt lgkmcnt(0)\n" \
  "s_barrier\n" \
  "s_memrealtime s[28:29]\n" \
  "s_lshr_b32 s101, s98, 2\n" \
  "ds_read_b128 v[8:11], v118 offset:0\n" \
  "ds_read_b128 v[12:15], v118 offset:1024\n" \
  "v_smfmac_f32_16x16x64_bf16 v[76:79], v[4:7], a[72:79], v125\n" \
  "ds_read_b128 v[16:19], v119 offset:0\n" \
  "ds_read_b128 v[20:23], v119 offset:1024\n" \
  "v_smfmac_f32_16x16x64_bf16 v[80:83], v[0:3], a[128:135], v125\n" \
  "ds_read_b128 v[24:27], v120 offset:0\n" \
  "ds_read_b128 v[28:31], v120 offset:1024\n" \
  "v_smfmac_f32_16x16x64_bf16 v[84:87], v[0:3], a[192:199], v125\n" \
  "v_smfmac_f32_16x16x64_bf16 v[80:83], v[4:7], a[136:143], v125\n" \
  "v_smfmac_f32_16x16x64_bf16 v[84:87], v[4:7], a[200:207], v125\n" \
  "s_waitcnt lgkmcnt(5)\n" \
  "v_smfmac_f32_16x16x64_bf16 v[64:67], v[8:11], v[144:151], v125\n" \
  "s_add_u32 s62, s101, s28\n" \
  "s_mov_b64 exec, 1\n" \
  "s_nop 0\n" \
  "v_mov_b32 v112, s62\n" \
  "buffer_store_dword v112, v113, s[40:43], s95 offen sc1\n" \
  "s_mov_b64 exec, -1\n" \
  "s_nop 0\n" \
  "v_smfmac_f32_16x16x64_bf16 v[68:71], v[8:11], v[208:215], v125\n" \
  "s_waitcnt lgkmcnt(4)\n" \
  "v_smfmac_f32_16x16x64_bf16 v[64:67], v[12:15], v[152:159], v125\n" \
  "v_smfmac_f32_16x16x64_bf16 v[68:71], v[12:15], v[216:223], v125\n" \
  "s_waitcnt lgkmcnt(3)\n" \
  "v_smfmac_f32_16x16x64_bf16 v[64:67], v[16:19], v[160:167], v125\n" \
  "v_smfmac_f32_16x16x64_bf16 v[68:71], v[16:19], v[224:231], v125\n" \
  "s_waitcnt lgkmcnt(2)\n" \
  "v_smfmac_f32_16x16x64_bf16 v[64:67], v[20:23], v[168:175], v125\n" \
  "v_smfmac_f32_16x16x64_bf16 v[68:71], v[20:23], v[232:239], v125\n" \
  "s_waitcnt lgkmcnt(1)\n" \
  "v_smfmac_f32_16x16x64_bf16 v[64:67], v[24:27], v[176:183], v125\n" \
  "v_smfmac_f32_16x16x64_bf16 v[68:71], v[24:27], v[240:247], v125\n" \
  "s_waitcnt lgkmcnt(0)\n" \
  "v_smfmac_f32_16x16x64_bf16 v[64:67], v[28:31], v[184:191], v125\n" \
  "v_smfmac_f32_16x16x64_bf16 v[68:71], v[28:31], v[248:255], v125\n" \
  "ds_read_b128 v[0:3], v121 offset:8192\n" \
  "ds_read_b128 v[4:7], v121 offset:9216\n" \
  "v_smfmac_f32_16x16x64_bf16 v[72:75], v[8:11], a[16:23], v125\n" \
  "v_cvt_pk_bf16_f32 v64, v64, v65\n" \
  "v_cvt_pk_bf16_f32 v65, v66, v67\n" \
  "v_smfmac_f32_16x16x64_bf16 v[76:79], v[8:11], a[80:87], v125\n" \
  "v_cvt_pk_bf16_f32 v68, v68, v69\n" \
  "v_cvt_pk_bf16_f32 v69, v70, v71\n" \
  "v_smfmac_f32_16x16x64_bf16 v[72:75], v[12:15], a[24:31], v125\n" \
  "v_mfma_f32_16x16x16_bf16 v[104:107], v[64:65], v[126:127], 0\n" \
  "v_mfma_f32_16x16x16_bf16 v[108:111], v[68:69], v[126:127], 0\n" \
  "v_smfmac_f32_16x16x64_bf16 v[76:79], v[12:15], a[88:95], v125\n" \
  "v_cvt_pk_bf16_f32 v104, v104, v105\n" \
  "v_cvt_pk_bf16_f32 v105, v106, v107\n" \
  "v_smfmac_f32_16x16x64_bf16 v[72:75], v[16:19], a[32:39], v125\n" \
  "v_cvt_pk_bf16_f32 v106, v108, v109\n" \
  "v_cvt_pk_bf16_f32 v107, v110, v111\n" \
  "v_smfmac_f32_16x16x64_bf16 v[76:79], v[16:19], a[96:103], v125\n" \
  "s_nop 0\n" \
  "v_mfma_f32_16x16x32_bf16 v[64:67], v[88:91], v[104:107], 0\n" \
  "v_mfma_f32_16x16x32_bf16 v[68:71], v[92:95], v[104:107], 0\n" \
  "v_smfmac_f32_16x16x64_bf16 v[72:75], v[20:23], a[40:47], v125\n" \
  "v_smfmac_f32_16x16x64_bf16 v[76:79], v[20:23], a[104:111], v125\n" \
  "v_cndmask_b32 v64, v68, v64, s[60:61]\n" \
  "v_cndmask_b32 v65, v69, v65, s[60:61]\n" \
  "v_smfmac_f32_16x16x64_bf16 v[72:75], v[24:27], a[48:55], v125\n" \
  "v_cndmask_b32 v66, v70, v66, s[60:61]\n" \
  "v_cndmask_b32 v67, v71, v67, s[60:61]\n" \
  "v_smfmac_f32_16x16x64_bf16 v[76:79], v[24:27], a[112:119], v125\n" \
  "ds_write_b128 v117, v[64:67] offset:8192\n" \
  "v_smfmac_f32_16x16x64_bf16 v[72:75], v[28:31], a[56:63], v125\n" \
  "v_smfmac_f32_16x16x64_bf16 v[76:79], v[28:31], a[120:127], v125\n" \
  "v_smfmac_f32_16x16x64_bf16 v[80:83], v[8:11], a[144:151], v125\n" \
  "v_cvt_pk_bf16_f32 v72, v72, v73\n" \
  "v_cvt_pk_bf16_f32 v73, v74, v75\n" \
  "v_cvt_pk_bf16_f32 v76, v76, v77\n" \
  "s_nop 0\n" \
  "v_mfma_f32_16x16x16_bf16 v[108:111], v[72:73], v[126:127], 0\n" \
  "v_cvt_pk_bf16_f32 v77, v78, v79\n" \
  "s_nop 1\n" \
  "v_mfma_f32_16x16x16_bf16 v[112:115], v[76:77], v[126:127], 0\n" \
  "v_smfmac_f32_16x16x64_bf16 v[84:87], v[8:11], a[208:215], v125\n" \
  "ds_read_b128 v[68:71], v118 offset:8192\n" \
  "ds_read_b128 v[88:91], v119 offset:8192\n" \
  "ds_read_b128 v[92:95], v120 offset:8192\n" \
  "v_cvt_pk_bf16_f32 v108, v108, v109\n" \
  "v_cvt_pk_bf16_f32 v109, v110, v111\n" \
  "v_cvt_pk_bf16_f32 v110, v112, v113\n" \
  "v_cvt_pk_bf16_f32 v111, v114, v115\n" \
  "s_cmp_eq_u32 s44, 63\n" \
  "s_cbranch_scc1 LAST%=\n" \
  "v_mfma_f32_16x16x32_bf16 v[72:75], v[108:111], v[104:107], 0\n" \
  "v_smfmac_f32_16x16x64_bf16 v[80:83], v[12:15], a[152:159], v125\n" \
  "s_waitcnt lgkmcnt(2)\n" \
  "v_add_f32 v64, v64, v68\n" \
  "v_add_f32 v65, v65, v69\n" \
  "v_cndmask_b32 v72, v72, v74, s[38:39]\n" \
  "v_cndmask_b32 v73, v73, v75, s[38:39]\n" \
  "ds_write_b64 v117, v[72:73] offset:9216\n" \
  "v_smfmac_f32_16x16x64_bf16 v[84:87], v[12:15], a[216:223], v125\n" \
  "s_waitcnt lgkmcnt(1)\n" \
  "v_add_f32 v66, v66, v70\n" \
  "v_add_f32 v67, v67, v71\n" \
  "v_add_f32 v88, v88, v92\n" \
  "v_smfmac_f32_16x16x64_bf16 v[80:83], v[16:19], a[160:167], v125\n" \
  "s_lshl_b32 s65, s54, 11\n" \
  "s_add_u32 m0, s65, 16384\n" \
  "v_add_f32 v89, v89, v93\n" \
  "v_add_f32 v90, v90, v94\n" \
  "v_add_f32 v91, v91, v95\n" \
  "v_smfmac_f32_16x16x64_bf16 v[84:87], v[16:19], a[224:231], v125\n" \
  "s_add_u32 s65, s52, 16777216\n" \
  "v_add_f32 v64, v64, v88\n" \
  "v_add_f32 v65, v65, v89\n" \
  "v_smfmac_f32_16x16x64_bf16 v[80:83], v[20:23], a[168:175], v125\n" \
  "v_add_f32 v66, v66, v90\n" \
  "v_add_f32 v67, v67, v91\n" \
  "v_smfmac_f32_16x16x64_bf16 v[84:87], v[20:23], a[232:239], v125\n" \
  "ds_read_b64 v[74:75], v118 offset:9216\n" \
  "ds_read_b64 v[76:77], v119 offset:9216\n" \
  "ds_read_b64 v[78:79], v120 offset:9216\n" \
  "v_max3_f32 v124, v64, v65, v66\n" \
  "v_max_f32 v124, v124, v67\n" \
  "v_smfmac_f32_16x16x64_bf16 v[80:83], v[24:27], a[176:183], v125\n" \
  "ds_read_b128 v[8:11], v121 offset:10240\n" \
  "ds_read_b128 v[12:15], v121 offset:11264\n" \
  "v_mov_b64 v[104:105], s[34:35]\n" \
  "v_mov_b64 v[106:107], s[34:35]\n" \
  "v_smfmac_f32_16x16x64_bf16 v[84:87], v[24:27], a[240:247], v125\n" \
  "ds_read_b128 v[16:19], v121 offset:12288\n" \
  "ds_read_b128 v[20:23], v121 offset:13312\n" \
  "v_max_f32_dpp v124, v124, v124 row_ror:8 row_mask:0xf bank_mask:0xf\n" \
  "v_mov_b64 v[92:93], s[36:37]\n" \
  "s_waitcnt lgkmcnt(4)\n" \
  "v_pk_add_f32 v[72:73], v[72:73], v[74:75]\n" \
  "v_pk_add_f32 v[76:77], v[76:77], v[78:79]\n" \
  "v_pk_add_f32 v[72:73], v[72:73], v[76:77]\n" \
  "v_max3_f32 v94, v72, v73, v124\n" \
  "v_max3_f32 v124, v72, v73, v124\n" \
  "v_lshlrev_b32 v75, 3, v117\n" \
  "v_smfmac_f32_16x16x64_bf16 v[80:83], v[28:31], a[184:191], v125\n" \
  "s_mov_b64 exec, 0xffff\n" \
  "buffer_load_dword v74, v75, s[40:43], s99 offen sc1\n" \
  "s_mov_b64 exec, -1\n" \
  "s_nop 0\n" \
  "v_permlane32_swap_b32 v124, v94\n" \
  "v_max_f32 v124, v124, v94\n" \
  "v_mov_b32 v94, v124\n" \
  "v_smfmac_f32_16x16x64_bf16 v[84:87], v[28:31], a[248:255], v125\n" \
  "s_nop 0\n" \
  "v_permlane16_swap_b32 v124, v94\n" \
  "v_max_f32 v124, v124, v94\n" \
  "v_mul_f32 v124, 0xbe0293ee, v124\n" \
  "v_fmamk_f32 v64, v64, 0x3e0293ee, v124\n" \
  "v_exp_f32 v64, v64\n" \
  "v_fmamk_f32 v65, v65, 0x3e0293ee, v124\n" \
  "v_fmamk_f32 v66, v66, 0x3e0293ee, v124\n" \
  "v_exp_f32 v65, v65\n" \
  "v_fmamk_f32 v67, v67, 0x3e0293ee, v124\n" \
  "v_fmamk_f32 v72, v72, 0x3e0293ee, v124\n" \
  "v_exp_f32 v66, v66\n" \
  "v_fmamk_f32 v73, v73, 0x3e0293ee, v124\n" \
  "v_cvt_pk_bf16_f32 v64, v64, v65\n" \
  "v_exp_f32 v67, v67\n" \
  "v_cvt_pk_bf16_f32 v112, v80, v81\n" \
  "v_cvt_pk_bf16_f32 v65, v66, v67\n" \
  "v_exp_f32 v72, v72\n" \
  "v_mov_b32_dpp v66, v64 row_ror:8 row_mask:0xf bank_mask:0xf\n" \
  "v_mov_b32_dpp v67, v65 row_ror:8 row_mask:0xf bank_mask:0xf\n" \
  "v_exp_f32 v73, v73\n" \
  "v_cvt_pk_bf16_f32 v113, v82, v83\n" \
  "v_mfma_f32_16x16x32_bf16 v[88:91], v[104:107], v[64:67], 0\n" \
  "v_cvt_pk_bf16_f32 v72, v72, v73\n" \
  "v_mov_b32 v73, v72\n" \
  "v_mfma_f32_16x16x32_bf16 v[76:79], v[96:99], v[64:67], 0\n" \
  "v_cvt_pk_bf16_f32 v114, v84, v85\n" \
  "v_permlane32_swap_b32 v72, v73\n" \
  "v_mfma_f32_16x16x32_bf16 v[68:71], v[100:103], v[64:67], 0\n" \
  "ds_read_b128 v[24:27], v121 offset:14336\n" \
  "ds_read_b128 v[28:31], v121 offset:15360\n" \
  "v_cvt_pk_bf16_f32 v115, v86, v87\n" \
  "v_mfma_f32_16x16x16_bf16 v[88:91], v[92:93], v[72:73], v[88:91]\n" \
  "buffer_load_dwordx4 v117, s[40:43], s52 offen sc1 lds\n" \
  "v_mfma_f32_16x16x16_bf16 v[76:79], v[112:113], v[72:73], v[76:79]\n" \
  "v_mfma_f32_16x16x16_bf16 v[68:71], v[114:115], v[72:73], v[68:71]\n" \
  "SFIN%=:\n" \
  "v_cmp_u_f32 vcc, v88, v88\n" \
  "v_rcp_f32 v88, v88\n" \
  "v_mov_b64 v[80:81], 0\n" \
  "v_mov_b64 v[82:83], 0\n" \
  "v_mov_b64 v[84:85], 0\n" \
  "v_mov_b64 v[86:87], 0\n" \
  "s_nop 0\n" \
  "v_cndmask_b32 v76, v68, v76, s[60:61]\n" \
  "v_cndmask_b32 v77, v69, v77, s[60:61]\n" \
  "v_cndmask_b32 v78, v70, v78, s[60:61]\n" \
  "v_cndmask_b32 v79, v71, v79, s[60:61]\n" \
  "v_pk_mul_f32 v[76:77], v[76:77], v[88:89] op_sel_hi:[1,0]\n" \
  "v_pk_mul_f32 v[78:79], v[78:79], v[88:89] op_sel_hi:[1,0]\n" \
  "v_cvt_pk_bf16_f32 v76, v76, v77\n" \
  "v_cvt_pk_bf16_f32 v77, v78, v79\n" \
  "s_cbranch_vccnz RETS%=\n" \
  "SOK%=:\n" \
  "ds_write_b64 v122, v[76:77]\n" \
  "buffer_load_dwordx4 v117, s[40:43], s52 offen offset:1024 sc1 lds\n" \
  "s_add_u32 s67, s44, 3\n" \
  "s_min_u32 s67, s67, 64\n" \
  "s_waitcnt lgkmcnt(0)\n" \
  "s_barrier\n" \
  "ds_read_b128 v[88:91], v116 offset:32768\n" \
  "ds_read_b128 v[92:95], v116 offset:33280\n" \
  "ds_read_b128 v[96:99], v116 offset:33792\n" \
  "ds_read_b128 v[100:103], v116 offset:34304\n" \
  "s_add_u32 m0, m0, 8192\n" \
  "s_nop 0\n" \
  "buffer_load_dwordx4 v117, s[40:43], s65 offen sc1 lds\n" \
  "s_waitcnt lgkmcnt(3)\n" \
  "v_smfmac_f32_16x16x64_bf16 v[80:83], v[88:91], v[32:39], v125\n" \
  "v_smfmac_f32_16x16x64_bf16 v[84:87], v[88:91], v[40:47], v125\n" \
  "s_waitcnt lgkmcnt(2)\n" \
  "v_smfmac_f32_16x16x64_bf16 v[80:83], v[92:95], v[48:55], v125\n" \
  "buffer_load_dwordx4 v117, s[40:43], s65 offen offset:1024 sc1 lds\n" \
  "v_smfmac_f32_16x16x64_bf16 v[84:87], v[92:95], v[56:63], v125\n" \
  "s_waitcnt lgkmcnt(1)\n" \
  "v_smfmac_f32_16x16x64_bf16 v[80:83], v[96:99], v[0:7], v125\n" \
  "s_waitcnt lgkmcnt(0)\n" \
  "v_smfmac_f32_16x16x64_bf16 v[80:83], v[100:103], v[16:23], v125\n" \
  "v_smfmac_f32_16x16x64_bf16 v[84:87], v[96:99], v[8:15], v125\n" \
  "v_smfmac_f32_16x16x64_bf16 v[84:87], v[100:103], v[24:31], v125\n" \
  "v_cvt_pk_bf16_f32 v104, v80, v81\n" \
  "v_cvt_pk_bf16_f32 v105, v82, v83\n" \
  "s_nop 3\n" \
  "v_cvt_pk_bf16_f32 v106, v84, v85\n" \
  "v_cvt_pk_bf16_f32 v107, v86, v87\n" \
  "s_waitcnt vmcnt(6)\n" \
  "buffer_store_dwordx4 v[104:107], v123, s[40:43], s46 offen sc1\n" \
  "s_memrealtime s[92:93]\n" \
  "ds_read_b128 v[32:35], v121 offset:0\n" \
  "ds_read_b128 v[36:39], v121 offset:1024\n" \
  "ds_read_b128 v[40:43], v121 offset:2048\n" \
  "ds_read_b128 v[44:47], v121 offset:3072\n" \
  "ds_read_b128 v[48:51], v121 offset:4096\n" \
  "ds_read_b128 v[52:55], v121 offset:5120\n" \
  "ds_read_b128 v[56:59], v121 offset:6144\n" \
  "ds_read_b128 v[60:63], v121 offset:7168\n" \
  "s_cmp_eq_u32 s58, 0\n" \
  "s_cbranch_scc1 KM0%=\n" \
  "v_mov_b64 v[108:109], v[112:113]\n" \
  "v_mov_b64 v[110:111], v[114:115]\n" \
  "KM0%=:\n" \
  "s_waitcnt lgkmcnt(0)\n" \
  "s_sub_u32 s62, s92, s28\n" \
  "s_lshl_b32 s62, s62, 2\n" \
  "s_sub_u32 s63, s98, 1\n" \
  "s_max_i32 s62, s62, s63\n" \
  "s_add_u32 s63, s98, 16\n" \
  "s_min_i32 s98, s62, s63\n" \
  "s_cmp_eq_u32 s44, 0\n" \
  "s_cselect_b32 s98, 320, s98\n" \
  "s_mov_b32 s28, s92\n" \
  "s_add_u32 s63, s44, 6\n" \
  "s_min_u32 s63, s63, 64\n" \
  "s_cmp_ge_u32 s48, s63\n" \
  "s_cbranch_scc1 POLLED1%=\n" \
  "v_mov_b32 v66, 0\n" \
  "ds_read_b128 v[64:67], v66 offset:36864\n" \
  "s_waitcnt lgkmcnt(0)\n" \
  "v_readfirstlane_b32 s63, v64\n" \
  "v_readfirstlane_b32 s64, v65\n" \
  "v_readfirstlane_b32 s65, v66\n" \
  "v_readfirstlane_b32 s66, v67\n" \
  "s_nop 3\n" \
  "s_sub_u32 s63, s63, s50\n" \
  "s_subb_u32 s64, s64, s51\n" \
  "s_cmp_eq_u32 s64, 0\n" \
  "s_cselect_b32 s63, s63, 0\n" \
  "s_cmp_le_u32 s63, 100\n" \
  "s_cselect_b32 s63, s63, 0\n" \
  "s_sub_u32 s65, s65, s50\n" \
  "s_subb_u32 s66, s66, s51\n" \
  "s_cmp_eq_u32 s66, 0\n" \
  "s_cselect_b32 s65, s65, 0\n" \
  "s_cmp_le_u32 s65, 100\n" \
  "s_cselect_b32 s65, s65, 0\n" \
  "s_min_u32 s63, s63, s65\n" \
  "s_max_u32 s48, s48, s63\n" \
  "s_cmp_ge_u32 s48, s67\n" \
  "s_cbranch_scc1 POLLED1%=\n" \
  "POLL1%=:\n" \
  "v_and_b32 v66, 16, v117\n" \
  "v_lshlrev_b32 v66, 8, v66\n" \
  "s_mov_b64 exec, 3\n" \
  "buffer_load_dwordx2 v[64:65], v66, s[40:43], s57 offen sc1\n" \
  "s_waitcnt vmcnt(0)\n" \
  "s_mov_b64 exec, -1\n" \
  "s_nop 0\n" \
  "v_readlane_b32 s63, v64, 0\n" \
  "v_readlane_b32 s64, v65, 0\n" \
  "v_readlane_b32 s65, v64, 1\n" \
  "v_readlane_b32 s66, v65, 1\n" \
  "s_nop 3\n" \
  "s_sub_u32 s63, s63, s50\n" \
  "s_subb_u32 s64, s64, s51\n" \
  "s_cmp_eq_u32 s64, 0\n" \
  "s_cselect_b32 s63, s63, 0\n" \
  "s_cmp_le_u32 s63, 100\n" \
  "s_cselect_b32 s63, s63, 0\n" \
  "s_sub_u32 s65, s65, s50\n" \
  "s_subb_u32 s66, s66, s51\n" \
  "s_cmp_eq_u32 s66, 0\n" \
  "s_cselect_b32 s65, s65, 0\n" \
  "s_cmp_le_u32 s65, 100\n" \
  "s_cselect_b32 s65, s65, 0\n" \
  "s_min_u32 s63, s63, s65\n" \
  "s_max_u32 s48, s48, s63\n" \
  "s_cmp_ge_u32 s48, s67\n" \
  "s_cbranch_scc1 POLLED1%=\n" \
  "s_sleep 1\n" \
  "s_sub_u32 s49, s49, 1\n" \
  "s_cbranch_scc0 POLL1%=\n" \
  "s_mov_b32 s49, 0\n" \
  "s_mov_b32 s48, 255\n" \
  "POLLED1%=:\n" \
  "s_waitcnt vmcnt(5)\n" \
  "TSC%=:\n" \
  "s_mov_b64 exec, 0xffff\n" \
  "s_nop 0\n" \
  "v_subrev_u32 v0, s69, v74\n" \
  "v_cmp_ge_i32 vcc, 0, v0\n" \
  "v_mov_b32 v1, 0\n" \
  "s_cbranch_vccnz RETT%=\n" \
  "TSOK%=:\n" \
  "ds_max_u32 v1, v74 offset:37376\n" \
  "s_mov_b64 exec, -1\n" \
  "s_nop 0\n" \
  "v_mov_b64 v[64:65], 0\n" \
  "v_mov_b64 v[66:67], 0\n" \
  "v_mov_b64 v[68:69], 0\n" \
  "v_mov_b64 v[70:71], 0\n" \
  "v_mov_b64 v[80:81], 0\n" \
  "v_mov_b64 v[82:83], 0\n" \
  "v_mov_b64 v[84:85], 0\n" \
  "v_mov_b64 v[86:87], 0\n" \
  "v_mov_b64 v[24:25], -1\n" \
  "v_mov_b64 v[26:27], -1\n" \
  "v_mov_b32 v113, s30\n" \
  "ds_write_b128 v117, v[24:27] offset:8192\n" \
  "ds_write_b64 v117, v[24:25] offset:9216\n" \
  "ds_read_b32 v28, v117 offset:9224\n" \
  "v_mov_b64 v[72:73], 0\n" \
  "v_mov_b64 v[74:75], 0\n" \
  "v_mov_b64 v[76:77], 0\n" \
  "v_mov_b64 v[78:79], 0\n" \
  "s_xor_b32 s95, s95, 16384\n" \
  "s_mov_b32 s46, s47\n" \
  "s_add_u32 s47, s47, 0x80000\n" \
  "s_sub_u32 s62, s47, s55\n" \
  "s_cmp_eq_u32 s62, 0x180000\n" \
  "s_cselect_b32 s47, s55, s47\n" \
  "s_add_u32 s44, s44, 1\n" \
  "s_and_b32 s62, s44, 3\n" \
  "s_lshl_b32 s62, s62, 14\n" \
  "s_add_u32 s100, s62, s45\n" \
  "s_mov_b32 s69, s28\n" \
  "v_mov_b32 v1, 0\n" \
  "s_waitcnt lgkmcnt(0)\n" \
  "s_barrier\n" \
  "ds_read_b32 v0, v1 offset:37376\n" \
  "s_waitcnt vmcnt(1)\n" \
  "ds_read_b128 v[88:91], v117 offset:16384\n" \
  "ds_read_b128 v[92:95], v117 offset:17408\n" \
  "ds_read_b128 v[96:99], v117 offset:24576\n" \
  "ds_read_b64 v[100:101], v117 offset:25608\n" \
  "ds_read_b64 v[102:103], v117 offset:25600\n" \
  "s_waitcnt lgkmcnt(5)\n" \
  "v_readfirstlane_b32 s62, v0\n" \
  "s_nop 3\n" \
  "s_add_u32 s97, s62, 6\n" \
  "s_add_u32 s68, s97, s31\n" \
  "s_mov_b32 s31, 40\n" \
  "s_branch JOIN%=\n" \
  "RETA%=:\n" \
  "s_sub_u32 s49, s49, 1\n" \
  "s_cbranch_scc1 RETAX%=\n" \
  "s_nop 7\n" \
  "s_nop 7\n" \
  "v_mov_b64 v[64:65], 0\n" \
  "v_mov_b64 v[66:67], 0\n" \
  "v_mov_b64 v[68:69], 0\n" \
  "v_mov_b64 v[70:71], 0\n" \
  "v_mov_b64 v[72:73], 0\n" \
  "v_mov_b64 v[74:75], 0\n" \
  "v_mov_b64 v[76:77], 0\n" \
  "v_mov_b64 v[78:79], 0\n" \
  "buffer_load_dwordx4 v[0:3], v117, s[40:43], s100 offen sc1\n" \
  "buffer_load_dwordx4 v[4:7], v117, s[40:43], s100 offen offset:1024 sc1\n" \
  "s_waitcnt vmcnt(0)\n" \
  "s_branch GA%=\n" \
  "RETAX%=:\n" \
  "s_mov_b32 s49, 0\n" \
  "s_branch GAOK%=\n" \
  "RETS%=:\n" \
  "s_sub_u32 s49, s49, 1\n" \
  "s_cbranch_scc1 RETSX%=\n" \
  "s_waitcnt lgkmcnt(0)\n" \
  "s_waitcnt vmcnt(0)\n" \
  "ds_read_b128 v[64:67], v117 offset:8192\n" \
  "ds_read_b128 v[68:71], v118 offset:8192\n" \
  "ds_read_b128 v[88:91], v119 offset:8192\n" \
  "ds_read_b128 v[92:95], v120 offset:8192\n" \
  "ds_read_b64 v[72:73], v117 offset:9216\n" \
  "ds_read_b64 v[74:75], v118 offset:9216\n" \
  "ds_read_b64 v[76:77], v119 offset:9216\n" \
  "ds_read_b64 v[78:79], v120 offset:9216\n" \
  "s_waitcnt lgkmcnt(0)\n" \
  "v_add_f32 v64, v64, v68\n" \
  "v_add_f32 v65, v65, v69\n" \
  "v_add_f32 v66, v66, v70\n" \
  "v_add_f32 v67, v67, v71\n" \
  "v_add_f32 v88, v88, v92\n" \
  "v_add_f32 v89, v89, v93\n" \
  "v_add_f32 v90, v90, v94\n" \
  "v_add_f32 v91, v91, v95\n" \
  "v_add_f32 v64, v64, v88\n" \
  "v_add_f32 v65, v65, v89\n" \
  "v_add_f32 v66, v66, v90\n" \
  "v_add_f32 v67, v67, v91\n" \
  "v_max3_f32 v124, v64, v65, v66\n" \
  "v_max_f32 v124, v124, v67\n" \
  "v_mov_b64 v[104:105], s[34:35]\n" \
  "v_mov_b64 v[106:107], s[34:35]\n" \
  "v_max_f32_dpp v124, v124, v124 row_ror:8 row_mask:0xf bank_mask:0xf\n" \
  "v_mov_b64 v[92:93], s[36:37]\n" \
  "v_pk_add_f32 v[72:73], v[72:73], v[74:75]\n" \
  "v_pk_add_f32 v[76:77], v[76:77], v[78:79]\n" \
  "v_pk_add_f32 v[72:73], v[72:73], v[76:77]\n" \
  "v_max3_f32 v94, v72, v73, v124\n" \
  "v_max3_f32 v124, v72, v73, v124\n" \
  "s_nop 1\n" \
  "v_permlane32_swap_b32 v124, v94\n" \
  "v_max_f32 v124, v124, v94\n" \
  "v_mov_b32 v94, v124\n" \
  "s_nop 1\n" \
  "v_permlane16_swap_b32 v124, v94\n" \
  "v_max_f32 v124, v124, v94\n" \
  "v_mul_f32 v124, 0xbe0293ee, v124\n" \
  "v_fmamk_f32 v64, v64, 0x3e0293ee, v124\n" \
  "v_exp_f32 v64, v64\n" \
  "v_fmamk_f32 v65, v65, 0x3e0293ee, v124\n" \
  "v_fmamk_f32 v66, v66, 0x3e0293ee, v124\n" \
  "v_exp_f32 v65, v65\n" \
  "v_fmamk_f32 v67, v67, 0x3e0293ee, v124\n" \
  "v_fmamk_f32 v72, v72, 0x3e0293ee, v124\n" \
  "v_exp_f32 v66, v66\n" \
  "v_fmamk_f32 v73, v73, 0x3e0293ee, v124\n" \
  "v_cvt_pk_bf16_f32 v64, v64, v65\n" \
  "v_exp_f32 v67, v67\n" \
  "s_nop 0\n" \
  "v_cvt_pk_bf16_f32 v65, v66, v67\n" \
  "v_exp_f32 v72, v72\n" \
  "v_mov_b32_dpp v66, v64 row_ror:8 row_mask:0xf bank_mask:0xf\n" \
  "v_mov_b32_dpp v67, v65 row_ror:8 row_mask:0xf bank_mask:0xf\n" \
  "v_exp_f32 v73, v73\n" \
  "s_nop 0\n" \
  "v_mfma_f32_16x16x32_bf16 v[88:91], v[104:107], v[64:67], 0\n" \
  "v_cvt_pk_bf16_f32 v72, v72, v73\n" \
  "v_mov_b32 v73, v72\n" \
  "v_mfma_f32_16x16x32_bf16 v[76:79], v[96:99], v[64:67], 0\n" \
  "s_nop 0\n" \
  "v_permlane32_swap_b32 v72, v73\n" \
  "v_mfma_f32_16x16x32_bf16 v[68:71], v[100:103], v[64:67], 0\n" \
  "s_nop 0\n" \
  "v_mfma_f32_16x16x16_bf16 v[88:91], v[92:93], v[72:73], v[88:91]\n" \
  "v_mfma_f32_16x16x16_bf16 v[76:79], v[112:113], v[72:73], v[76:79]\n" \
  "v_mfma_f32_16x16x16_bf16 v[68:71], v[114:115], v[72:73], v[68:71]\n" \
  "v_mov_b32 v74, s69\n" \
  "s_waitcnt lgkmcnt(0)\n" \
  "s_nop 7\n" \
  "s_branch SFIN%=\n" \
  "RETSX%=:\n" \
  "s_mov_b32 s49, 0\n" \
  "s_branch SOK%=\n" \
  "RETT%=:\n" \
  "s_sub_u32 s49, s49, 1\n" \
  "s_cbranch_scc1 RETTX%=\n" \
  "v_lshlrev_b32 v0, 3, v117\n" \
  "s_sleep 2\n" \
  "buffer_load_dword v74, v0, s[40:43], s99 offen sc1\n" \
  "s_waitcnt vmcnt(0)\n" \
  "s_branch TSC%=\n" \
  "RETTX%=:\n" \
  "s_mov_b32 s49, 0\n" \
  "s_branch TSOK%=\n" \
  "JOIN%=:\n" \
  "s_memrealtime s[92:93]\n" \
  "s_waitcnt lgkmcnt(0)\n" \
  "s_sub_u32 s65, s68, s92\n" \
  "s_add_u32 s66, s92, 0\n" \
  "s_cmp_lt_i32 s65, 0\n" \
  "s_cselect_b32 s67, s66, s68\n" \
  "s_add_u32 s66, s92, 2\n" \
  "s_add_u32 s65, s65, 12\n" \
  "s_cmp_gt_u32 s65, 1000\n" \
  "s_cselect_b32 s67, s66, s67\n" \
  "s_branch WC2%=\n" \
  "WU2%=:\n" \
  "s_memrealtime s[92:93]\n" \
  "s_waitcnt lgkmcnt(0)\n" \
  "WC2%=:\n" \
  "s_sub_u32 s65, s67, s92\n" \
  "s_cmp_gt_i32 s65, 3\n" \
  "s_cbranch_scc1 WU2%=\n" \
  "s_cmp_lt_i32 s65, 1\n" \
  "s_cbranch_scc1 WX2%=\n" \
  "s_nop 3\n" \
  "s_cmp_lt_i32 s65, 2\n" \
  "s_cbranch_scc1 WX2%=\n" \
  "s_nop 3\n" \
  "s_cmp_lt_i32 s65, 3\n" \
  "s_cbranch_scc1 WX2%=\n" \
  "s_nop 3\n" \
  "WX2%=:\n" \
  "s_branch LOOP%=\n" \
  "LAST%=:\n" \
  "ds_read_b32 v88, v117 offset:9224\n" \
  "v_smfmac_f32_16x16x64_bf16 v[80:83], v[12:15], a[152:159], v125\n" \
  "v_smfmac_f32_16x16x64_bf16 v[84:87], v[12:15], a[216:223], v125\n" \
  "v_smfmac_f32_16x16x64_bf16 v[80:83], v[16:19], a[160:167], v125\n" \
  "v_smfmac_f32_16x16x64_bf16 v[84:87], v[16:19], a[224:231], v125\n" \
  "v_smfmac_f32_16x16x64_bf16 v[80:83], v[20:23], a[168:175], v125\n" \
  "v_smfmac_f32_16x16x64_bf16 v[84:87], v[20:23], a[232:239], v125\n" \
  "v_smfmac_f32_16x16x64_bf16 v[80:83], v[24:27], a[176:183], v125\n" \
  "v_smfmac_f32_16x16x64_bf16 v[84:87], v[24:27], a[240:247], v125\n" \
  "v_smfmac_f32_16x16x64_bf16 v[80:83], v[28:31], a[184:191], v125\n" \
  "v_smfmac_f32_16x16x64_bf16 v[84:87], v[28:31], a[248:255], v125\n" \
  "s_nop 3\n" \
  "v_cvt_pk_bf16_f32 v112, v80, v81\n" \
  "v_cvt_pk_bf16_f32 v113, v82, v83\n" \
  "s_nop 1\n" \
  "v_cvt_pk_bf16_f32 v114, v84, v85\n" \
  "v_cvt_pk_bf16_f32 v115, v86, v87\n" \
  "s_cmp_eq_u32 s58, 0\n" \
  "s_cbranch_scc1 KM3%=\n" \
  "v_mov_b64 v[108:109], v[112:113]\n" \
  "v_mov_b64 v[110:111], v[114:115]\n" \
  "KM3%=:\n" \
  "s_waitcnt lgkmcnt(0)\n" \
  "buffer_store_dwordx4 v[108:111], v88, s[40:43], s53 offen sc1\n" \
  "s_waitcnt vmcnt(0) lgkmcnt(0)\n"
#define RED_ASM \
  "s_mov_b32 s31, 0\n" \
  "s_mov_b32 s68, s48\n" \
  "s_mov_b32 s69, s68\n" \
  "s_add_u32 s97, s68, 50\n" \
  "s_mov_b32 s98, 170\n" \
  "s_sub_u32 s52, s68, 1000000\n" \
  "s_mov_b32 s53, s68\n" \
  "s_sub_u32 s28, s68, 10\n" \
  "s_add_u32 s95, s55, 1638400\n" \
  "s_sub_u32 s52, s68, 50000\n" \
  "s_mov_b32 s31, 40\n" \
  "v_mov_b64 v[24:25], -1\n" \
  "v_mov_b64 v[26:27], -1\n" \
  "LOOP%=:\n" \
  "s_add_u32 s67, s68, 78\n" \
  "s_memrealtime s[92:93]\n" \
  "s_waitcnt lgkmcnt(0)\n" \
  "s_sub_u32 s65, s67, s92\n" \
  "s_add_u32 s66, s92, 38\n" \
  "s_cmp_lt_i32 s65, 38\n" \
  "s_cselect_b32 s67, s66, s67\n" \
  "s_add_u32 s66, s92, 78\n" \
  "s_add_u32 s65, s65, 12\n" \
  "s_cmp_gt_u32 s65, 1000\n" \
  "s_cselect_b32 s67, s66, s67\n" \
  "s_branch WC0%=\n" \
  "WU0%=:\n" \
  "s_memrealtime s[92:93]\n" \
  "s_waitcnt lgkmcnt(0)\n" \
  "WC0%=:\n" \
  "s_sub_u32 s65, s67, s92\n" \
  "s_cmp_gt_i32 s65, 3\n" \
  "s_cbranch_scc1 WU0%=\n" \
  "s_cmp_lt_i32 s65, 1\n" \
  "s_cbranch_scc1 WX0%=\n" \
  "s_nop 3\n" \
  "s_cmp_lt_i32 s65, 2\n" \
  "s_cbranch_scc1 WX0%=\n" \
  "s_nop 3\n" \
  "s_cmp_lt_i32 s65, 3\n" \
  "s_cbranch_scc1 WX0%=\n" \
  "s_nop 3\n" \
  "WX0%=:\n" \
  "TSR%=:\n" \
  "buffer_load_dword v20, v79, s[40:43], s95 offen sc1\n" \
  "s_waitcnt vmcnt(0)\n" \
  "v_subrev_u32 v21, s52, v20\n" \
  "v_cmp_ge_i32 vcc, 0, v21\n" \
  "s_cbranch_vccnz RETT%=\n" \
  "TSOK%=:\n" \
  "v_max_u32_dpp v20, v20, v20 row_shr:1 row_mask:0xf bank_mask:0xf\n" \
  "s_nop 1\n" \
  "v_max_u32_dpp v20, v20, v20 row_shr:2 row_mask:0xf bank_mask:0xf\n" \
  "s_nop 1\n" \
  "v_max_u32_dpp v20, v20, v20 row_shr:4 row_mask:0xf bank_mask:0xf\n" \
  "s_nop 1\n" \
  "v_max_u32_dpp v20, v20, v20 row_shr:8 row_mask:0xf bank_mask:0xf\n" \
  "s_nop 1\n" \
  "v_max_u32_dpp v20, v20, v20 row_bcast:15 row_mask:0xf bank_mask:0xf\n" \
  "s_nop 1\n" \
  "v_max_u32_dpp v20, v20, v20 row_bcast:31 row_mask:0xf bank_mask:0xf\n" \
  "s_nop 0\n" \
  "v_readlane_b32 s62, v20, 63\n" \
  "s_nop 3\n" \
  "s_add_u32 s97, s62, 6\n" \
  "s_add_u32 s68, s97, s31\n" \
  "s_mov_b32 s31, 40\n" \
  "s_add_u32 s62, s44, 1\n" \
  "s_and_b32 s62, s62, 3\n" \
  "s_lshl_b32 s62, s62, 14\n" \
  "s_add_u32 s100, s62, s45\n" \
  "s_xor_b32 s29, s100, 0x8000\n" \
  "s_memrealtime s[92:93]\n" \
  "s_waitcnt lgkmcnt(0)\n" \
  "s_sub_u32 s65, s97, s92\n" \
  "s_add_u32 s66, s92, 0\n" \
  "s_cmp_lt_i32 s65, 0\n" \
  "s_cselect_b32 s94, s66, s97\n" \
  "s_add_u32 s66, s92, 0\n" \
  "s_add_u32 s65, s65, 12\n" \
  "s_cmp_gt_u32 s65, 1000\n" \
  "s_cselect_b32 s94, s66, s94\n" \
  "s_branch WC1%=\n" \
  "WU1%=:\n" \
  "s_memrealtime s[92:93]\n" \
  "s_waitcnt lgkmcnt(0)\n" \
  "WC1%=:\n" \
  "s_sub_u32 s65, s94, s92\n" \
  "s_cmp_gt_i32 s65, 3\n" \
  "s_cbranch_scc1 WU1%=\n" \
  "s_cmp_lt_i32 s65, 1\n" \
  "s_cbranch_scc1 WX1%=\n" \
  "s_nop 3\n" \
  "s_cmp_lt_i32 s65, 2\n" \
  "s_cbranch_scc1 WX1%=\n" \
  "s_nop 3\n" \
  "s_cmp_lt_i32 s65, 3\n" \
  "s_cbranch_scc1 WX1%=\n" \
  "s_nop 3\n" \
  "WX1%=:\n" \
  "buffer_load_dwordx4 v[0:3], v76, s[40:43], s46 offen offset:0 sc1\n" \
  "buffer_load_dwordx4 v[4:7], v76, s[40:43], s46 offen offset:1024 sc1\n" \
  "buffer_load_dwordx4 v[8:11], v76, s[40:43], s46 offen offset:2048 sc1\n" \
  "buffer_load_dwordx4 v[12:15], v76, s[40:43], s46 offen offset:3072 sc1\n" \
  "GB%=:\n" \
  "s_waitcnt vmcnt(3)\n" \
  "v_mfma_f32_16x16x32_bf16 v[16:19], v[0:3], v[72:75], 0\n" \
  "v_max3_u32 v31, v0, v1, v2\n" \
  "s_waitcnt vmcnt(2)\n" \
  "v_mfma_f32_16x16x32_bf16 v[16:19], v[4:7], v[72:75], v[16:19]\n" \
  "v_max3_u32 v31, v31, v3, v4\n" \
  "v_max3_u32 v31, v31, v5, v6\n" \
  "s_waitcnt vmcnt(1)\n" \
  "v_mfma_f32_16x16x32_bf16 v[16:19], v[8:11], v[72:75], v[16:19]\n" \
  "v_max3_u32 v31, v31, v7, v8\n" \
  "v_max3_u32 v31, v31, v9, v10\n" \
  "v_max_u32 v31, v31, v11\n" \
  "s_waitcnt vmcnt(0)\n" \
  "v_mfma_f32_16x16x32_bf16 v[16:19], v[12:15], v[72:75], v[16:19]\n" \
  "v_max3_u32 v30, v12, v13, v14\n" \
  "v_max3_u32 v31, v31, v30, v15\n" \
  "v_cmp_eq_u32 vcc, -1, v31\n" \
  "s_nop 3\n" \
  "v_add_f32 v16, v16, v18\n" \
  "v_add_f32 v17, v17, v19\n" \
  "v_cvt_pk_bf16_f32 v16, v16, v17\n" \
  "v_mov_b32 v17, v16\n" \
  "s_nop 0\n" \
  "v_mov_b32_dpp v18, v16 row_shl:4 row_mask:0xf bank_mask:0xf\n" \
  "v_mov_b32_dpp v19, v16 row_shl:4 row_mask:0xf bank_mask:0xf\n" \
  "v_permlane16_swap_b32 v16, v17\n" \
  "s_nop 0\n" \
  "v_permlane16_swap_b32 v18, v19\n" \
  "s_cbranch_vccnz RETB%=\n" \
  "GBOK%=:\n" \
  "buffer_store_dwordx4 v[16:19], v77, s[40:43], s100 offen sc1\n" \
  "s_memrealtime s[92:93]\n" \
  "buffer_store_dwordx4 v[24:27], v78, s[40:43], s29 offen sc1\n" \
  "s_xor_b32 s95, s95, 16384\n" \
  "s_add_u32 s46, s46, 0x80000\n" \
  "s_sub_u32 s62, s46, s55\n" \
  "s_cmp_eq_u32 s62, 0x180000\n" \
  "s_cselect_b32 s46, s55, s46\n" \
  "s_waitcnt lgkmcnt(0)\n" \
  "s_mov_b32 s52, s92\n" \
  "s_add_u32 s44, s44, 1\n" \
  "s_cmp_lg_u32 s44, 63\n" \
  "s_cbranch_scc1 LOOP%=\n" \
  "s_branch END%=\n" \
  "RETB%=:\n" \
  "s_sub_u32 s49, s49, 1\n" \
  "s_cbranch_scc1 RETBX%=\n" \
  "s_nop 7\n" \
  "buffer_load_dwordx4 v[0:3], v76, s[40:43], s46 offen offset:0 sc1\n" \
  "buffer_load_dwordx4 v[4:7], v76, s[40:43], s46 offen offset:1024 sc1\n" \
  "buffer_load_dwordx4 v[8:11], v76, s[40:43], s46 offen offset:2048 sc1\n" \
  "buffer_load_dwordx4 v[12:15], v76, s[40:43], s46 offen offset:3072 sc1\n" \
  "s_waitcnt vmcnt(0)\n" \
  "s_branch GB%=\n" \
  "RETBX%=:\n" \
  "s_mov_b32 s49, 0\n" \
  "s_branch GBOK%=\n" \
  "RETT%=:\n" \
  "s_sub_u32 s49, s49, 1\n" \
  "s_cbranch_scc1 RETTX%=\n" \
  "s_sleep 2\n" \
  "s_branch TSR%=\n" \
  "RETTX%=:\n" \
  "s_mov_b32 s49, 0\n" \
  "s_branch TSOK%=\n" \
  "END%=:\n" \
  "s_waitcnt vmcnt(0) lgkmcnt(0)\n"
#define CLOBBERS "v0", "v1", "v2", "v3", "v4", "v5", "v6", "v7", "v8", "v9", "v10", "v11", "v12", "v13", "v14", "v15", "v16", "v17", "v18", "v19", "v20", "v21", "v22", "v23", "v24", "v25", "v26", "v27", "v28", "v29", "v30", "v31", "v32", "v33", "v34", "v35", "v36", "v37", "v38", "v39", "v40", "v41", "v42", "v43", "v44", "v45", "v46", "v47", "v48", "v49", "v50", "v51", "v52", "v53", "v54", "v55", "v56", "v57", "v58", "v59", "v60", "v61", "v62", "v63", "v64", "v65", "v66", "v67", "v68", "v69", "v70", "v71", "v72", "v73", "v74", "v75", "v76", "v77", "v78", "v79", "v80", "v81", "v82", "v83", "v84", "v85", "v86", "v87", "v88", "v89", "v90", "v91", "v92", "v93", "v94", "v95", "v96", "v97", "v98", "v99", "v100", "v101", "v102", "v103", "v104", "v105", "v106", "v107", "v108", "v109", "v110", "v111", "s28", "s29", "s30", "s31", "s34", "s35", "s36", "s37", "s38", "s39", "s60", "s61", "s62", "s63", "s64", "s65", "s66", "s67", "s68", "s69", "s70", "s71", "s72", "s73", "s74", "s75", "s76", "s77", "s78", "s79", "s80", "s81", "s82", "s83", "s84", "s85", "s86", "s87", "s88", "s89", "s90", "s91", "s92", "s93", "s94", "s95", "s96", "s97", "s98", "s99", "s100", "s101"
#define RCLOBBERS "v0", "v1", "v2", "v3", "v4", "v5", "v6", "v7", "v8", "v9", "v10", "v11", "v12", "v13", "v14", "v15", "v16", "v17", "v18", "v19", "v20", "v21", "v22", "v23", "v24", "v25", "v26", "v27", "v28", "v29", "v30", "v31", "s28", "s29", "s30", "s31", "s60", "s61", "s62", "s63", "s64", "s65", "s66", "s67", "s68", "s69", "s70", "s71", "s72", "s73", "s74", "s75", "s76", "s77", "s78", "s79", "s80", "s81", "s82", "s83", "s84", "s85", "s86", "s87", "s88", "s89", "s90", "s91", "s92", "s93", "s94", "s95", "s96", "s97", "s98", "s99", "s100", "s101"
constexpr unsigned L_S = 8192, L_OX = 32768, L_C = 34816, L_CNT = 36864, L_WO = 65536;

#define TSO (n * 128)
#define T0D 110
DEVI unsigned wait_ready(rsrc_t rs, u64 nonce, int lane, int& budget) {
  v4u f;
  while (true) {
    asm volatile("" ::: "memory");
    f = ld4(rs, F_RDY + lane * 128, 0);
    if (__all((((u64)f.y << 32) | f.x) == nonce + 1) || --budget < 0) break;
    __builtin_amdgcn_s_sleep(1);
  }
  unsigned t = f.z;
  for (int o = 32; o; o >>= 1) t = max(t, (unsigned)__shfl_xor((int)t, o));
  return rfl(t) + T0D;
}

DEVI void role_red(const Params& p, rsrc_t rs, u64 nonce, int n, int lane) {
  int budget = SPIN;
  const int g = lane >> 4, c = lane & 15;
  const unsigned sel = (wait_flags(rs, lane == 0 ? F_SEL : OOB, nonce, 1, budget) == 2) ? 8192u : 0u;
  wait_flags(rs, F_INIT + lane * 8, nonce, 1, budget);
  const unsigned t0 = (unsigned)__builtin_amdgcn_s_memrealtime();
  v8u vv;
#pragma unroll
  for (int i = 0; i < 4; i++) vv[i] = (c & 7) == 2 * i ? 0x3F80u : (c & 7) == 2 * i + 1 ? 0x3F800000u : 0u;
  vv[4] = (n >> 1) * 16384u + (n & 1) * 4096u + (2 * g + ((c >> 1) & 1)) * 128 + (2 * (c >> 2) + (c & 1)) * 16;
  vv[5] = (c < 4 && !(g & 1)) ? n * 128u + (2 * g + c) * 16 : OOB;
  vv[6] = lane < 8 ? n * 128 + lane * 16 : OOB;
  vv[7] = lane * 128;
  v16u ss;
  ss[0] = (unsigned)(uintptr_t)p.ws; ss[1] = (unsigned)((uintptr_t)p.ws >> 32) & 0xffffu; ss[2] = WS_BYTES; ss[3] = 0x00020000u;
  ss[5] = sel + 96 * 16384;
  ss[4] = 0; ss[6] = sel; ss[7] = 0;
  ss[8] = t0; ss[9] = rfl(budget < 0 ? 0 : budget); ss[10] = 0; ss[11] = 0;
  ss[12] = 0; ss[13] = 0; ss[14] = 0; ss[15] = sel;
  v4u s2 = {0, 0, 0, 106 * MBy + n * 16384u};
  asm volatile(RED_ASM
               : "+{v[72:79]}"(vv), "+{s[40:55]}"(ss), "+{s[56:59]}"(s2)
               :
               : "memory", "vcc", "scc", RCLOBBERS);
}

DEVI void role_chain(const Params& p, rsrc_t rs, u64 nonce, int n, int lane, int w) {
  int budget = SPIN;
  const int h = n >> 1, ch = n & 1, g = lane >> 4, c = lane & 15;
  const unsigned rho_out = ch * 32 + w * 8 + (g >> 1) * 4 + (c >> 2);
  const unsigned v_pb = (rho_out >> 1) * 16384 + (rho_out & 1) * 4096 + h * 128 + ((g & 1) * 4 + (c & 3)) * 16;
  const unsigned v_rst = lane < 8 ? n * 128 + lane * 16 : OOB;
  {
    const v4u sv = {SENT, SENT, SENT, SENT};
#pragma unroll
    for (int q = 0; q < 6; q++) st4(sv, rs, v_pb, (q >> 1) * 32 * 16384 + (q & 1) * 8192);
    v4u m0 = sv;
    if (w == 0) {
      m0 = __builtin_amdgcn_raw_buffer_load_b128(mkrs(p.mem0, 8192), lane < 8 ? lane * 1024 + n * 16 : OOB, 0, 0);
#pragma unroll
      for (int i = 0; i < 4; i++) m0[i] = min(m0[i], SENT - 1);
    }
    st4(m0, rs, v_rst, (96 + w) * 16384);
    st4(m0, rs, v_rst, (96 + w) * 16384 + 8192);
    __builtin_amdgcn_raw_buffer_store_b32((unsigned)__builtin_amdgcn_s_memrealtime() - 100000u, rs, lane == 0 ? TSO : OOB, 100 * 16384 + w * 8192, SC1);
  }
  v32u W[12];
  unsigned known, sel;
  v2u fa, fb;
  {
    rsrc_t rq = mkrs(p.Wq, 512 * 4096 * 2), rk = mkrs(p.Wk, 512 * 4096 * 2), rv = rq, ro = rq;
    unsigned va[4], vu[4], vb[2];
    {
      const int r = lane >> 4, cc = lane & 15;
#pragma unroll
      for (int q = 0; q < 4; q++) {
        int unit = (cc >> 1) ^ ((r & 1) | (q << 1));
        va[q] = r * 8192 + h * 256 + unit * 32 + (cc & 1) * 16;
        vu[q] = (unit >> 2) * 2;
      }
      const int r2 = lane >> 5, c2 = lane & 31;
#pragma unroll
      for (int q = 0; q < 2; q++) vb[q] = r2 * 1024 + ch * 512 + (((c2 >> 2) ^ (r2 | (q << 1))) * 64) + (c2 & 3) * 16;
    }
    auto issue = [&](int i) {
      unsigned L = (i % 5) * 32768;
      if (i < 12) {
#pragma unroll
        for (int q = 0; q < 8; q++)
          dma(i < 4 ? rq : i < 8 ? rk : rv, va[q & 3] + (((i + vu[q & 3]) & 3) << 20), (w * 32 + q * 4) * 8192, L + (w * 32 + q * 4) * 256);
      } else {
#pragma unroll
        for (int q = 0; q < 8; q++) dma(ro, vb[(q >> 1) & 1], (h * 128 + (i - 12) * 64 + w * 16 + q * 2) * 1024, L + (w * 16 + q * 2) * 512);
      }
    };
#pragma unroll
    for (int i = 0; i < 5; i++) issue(i);
    const int r = (lane >> 2) & 3, cq = lane & 3;
    const int rl = (r & 1) | ((g & 1) << 1) | ((r >> 1) << 2);
    const unsigned rowb = 16 * (g >> 1) + 4 * (g & 1) + (r & 1) + 8 * (r >> 1);
    v8u Wt[8];
#pragma unroll
    for (int i = 0; i < 14; i++) {
      if (i == 3 || i == 7) {
        auto* kp = (const __attribute__((address_space(4))) Params*)__builtin_amdgcn_kernarg_segment_ptr();
        asm volatile("" : "+s"(kp));
        if (i == 3) rv = mkrs(kp->Wv, 512 * 4096 * 2); else ro = mkrs(kp->Wo, 512 * 4096 * 2);
      }
      if (i < 12 && (i & 3) == 0) continue;
      if (i == 10 || (i < 12 && (i & 3) == 1)) vmw<24>(); else if (i <= 9) vmw<32>(); else if (i == 11) vmw<16>(); else if (i == 12) vmw<8>(); else vmw<0>();
      BAR();
      if (i == 1 && w == 0) st_flag(rs, F_INIT + n * 8, nonce + 1);
      if (i == 13) {
        while (true) {
          const u64 d0 = (((u64)fa.y << 32) | fa.x) - nonce, d1 = (((u64)fb.y << 32) | fb.x) - nonce;
          known = min(__builtin_amdgcn_readlane((unsigned)d1, 0), __builtin_amdgcn_readlane((unsigned)d1, 1));
          sel = __builtin_amdgcn_readlane((unsigned)d1, 2) == 2 ? 8192u : 0u;
          if (__all(d0 == 1 && (lane >= 3 || d1 - 1 < 100)) || --budget < 0) break;
          __builtin_amdgcn_s_sleep(1);
          asm volatile("" ::: "memory");
          fa = __builtin_amdgcn_raw_buffer_load_b64(rs, F_INIT + lane * 8, 0, SC1);
          fb = __builtin_amdgcn_raw_buffer_load_b64(rs, lane < 2 ? F_PROJ + (lane * 32 + h) * 128 : lane == 2 ? F_SEL : OOB, 0, SC1);
        }
#pragma unroll
        for (int q = 0; q < 4; q++) dma_c(rs, lane * 16, (q < 2 ? O_KX : O_VX) + h * 8192 + w * 2048 + (q & 1) * 1024, 16384 + (q >> 1) * 8192 + w * 2048 + (q & 1) * 1024);
      }
      unsigned L = (i % 5) * 32768;
      if (i < 12) {
#pragma unroll
        for (int e = (i & 3) == 1 ? i - 1 : i; e <= i; e++)
#pragma unroll
        for (int T = 0; T < 2; T++)
#pragma unroll
          for (int tl = 0; tl < 2; tl++) {
            const unsigned Le = (i & 3) != 1 ? L : (w & 1) ? ((2 * i - 1 - e) % 5) * 32768 : (e % 5) * 32768;
            unsigned a = Le + (rowb + 64 * tl) * 256 + (((2 * w + T) ^ rl) * 32) + cq * 8;
            v2u t0 = trd(a), t1 = trd(a + 512), t2 = trd(a + 8192), t3 = trd(a + 8192 + 512);
            const int t = 2 * (e & 3) + tl, wi = (i >> 2) * 4 + T * 2 + (t >> 2), o = (t & 3) * 8;
            W[wi][o] = t0.x; W[wi][o + 1] = t0.y; W[wi][o + 2] = t1.x; W[wi][o + 3] = t1.y;
            W[wi][o + 4] = t2.x; W[wi][o + 5] = t2.y; W[wi][o + 6] = t3.x; W[wi][o + 7] = t3.y;
          }
      } else {
#pragma unroll
        for (int j = 0; j < 4; j++) {
          unsigned a = L + (32 * (j >> 1) + 4 * g + (r & 1)) * 512 + (((2 * w + (r >> 1)) ^ (rl & 3)) * 64) + cq * 16 + (j & 1) * 8;
          v2u t0 = trd(a), t1 = trd(a + 1024), t2 = trd(a + 8192), t3 = trd(a + 8192 + 1024);
          Wt[(i - 12) * 4 + j] = v8u{t0.x, t0.y, t1.x, t1.y, t2.x, t2.y, t3.x, t3.y};
        }
      }
      BAR();
      if (i < 12 && (i & 3) == 1) issue(i + 4);
      if (i + 5 < 14) issue(i + 5);
      if (i == 11) {
        fa = __builtin_amdgcn_raw_buffer_load_b64(rs, F_INIT + lane * 8, 0, SC1);
        fb = __builtin_amdgcn_raw_buffer_load_b64(rs, lane < 2 ? F_PROJ + (lane * 32 + h) * 128 : lane == 2 ? F_SEL : OOB, 0, SC1);
      }
    }
#pragma unroll
    for (int j = 0; j < 8; j++) {
      unsigned a = L_WO + w * 16384 + j * 2048 + lane * 16;
      *LDSP(v4u, a) = v4u{Wt[j][0], Wt[j][1], Wt[j][2], Wt[j][3]};
      *LDSP(v4u, a + 1024) = v4u{Wt[j][4], Wt[j][5], Wt[j][6], Wt[j][7]};
    }
    if (w == 1) *LDSP(v4u, L_CNT + lane * 16) = lane ? v4u{0, 0, 0, 0} : v4u{(unsigned)nonce, (unsigned)(nonce >> 32), (unsigned)nonce, (unsigned)(nonce >> 32)};
  }
  const unsigned l16 = lane * 16, t0 = 0;
  if (known < 2 || known > 100) known = wait_flags(rs, lane < 2 ? F_PROJ + (lane * 32 + h) * 128 : OOB, nonce, 2, budget);
  v16u vv;
  {
    const int d = (c & 7) - 4 * (g & 1);
    vv[0] = vv[1] = vv[2] = vv[3] = 0;
    vv[4] = (g * 8 + (c & 7)) * 16;
#pragma unroll
    for (int o = 0; o < 4; o++) vv[5 + o] = l16 + (((w & 1) && o ? (o == 1 ? w - 1 : w + o - 1) : w + o) & 3) * 2048;
    vv[9] = L_WO + w * 16384 + l16;
    vv[10] = L_OX + w * 512 + (8 * (2 * (c >> 3) + (g >> 1)) + (c & 7)) * 16 + (g & 1) * 8;
    vv[11] = v_pb;
    vv[12] = 0;
    *LDSP(unsigned, L_S + 1032 + w * 2048 + l16) = ch == 0 ? (c < 8 ? w * 512 + (g * 8 + c) * 16 : OOB) : 4096 + w * 1024 + l16;
    vv[13] = c < 8 ? 0x4444 : 0xEEEE;
    vv[14] = d == 0 ? 0x3F80u : d == 1 ? 0x3F800000u : 0u;
    vv[15] = d == 2 ? 0x3F80u : d == 3 ? 0x3F800000u : 0u;
  }
  v16u ss;
  ss[0] = (unsigned)(uintptr_t)p.ws; ss[1] = (unsigned)((uintptr_t)p.ws >> 32) & 0xffffu; ss[2] = WS_BYTES; ss[3] = 0x00020000u;
  ss[4] = 0;
  ss[5] = sel + 96 * 16384;
  ss[6] = sel;
  ss[7] = t0;

  ss[8] = known; ss[9] = rfl(budget < 0 ? 0 : budget); ss[10] = (unsigned)nonce; ss[11] = (unsigned)(nonce >> 32);
  ss[12] = O_KX + h * 8192; ss[13] = O_KM - 262144 + h * 8192; ss[14] = w; ss[15] = sel;
  v4u s2 = {F_CH + n * 128u, F_PROJ + h * 128u, (unsigned)ch, (n == 5 || n == 40) ? O_DBG + 32768 + (n == 40) * 32768u : 0x7f000000u};
  vmw<0>();
  BAR();
  asm volatile(CHAIN_ASM
               : "+{v[112:127]}"(vv), "+{s[40:55]}"(ss), "+{s[56:59]}"(s2)
               : "{v[128:159]}"(W[0]), "{v[160:191]}"(W[1]), "{v[192:223]}"(W[2]), "{v[224:255]}"(W[3]),
                 "{a[0:31]}"(W[4]), "{a[32:63]}"(W[5]), "{a[64:95]}"(W[6]), "{a[96:127]}"(W[7]),
                 "{a[128:159]}"(W[8]), "{a[160:191]}"(W[9]), "{a[192:223]}"(W[10]), "{a[224:255]}"(W[11])
               : "memory", "vcc", "scc", CLOBBERS);
  BAR();
  {
    rsrc_t rs = mkrs((const void*)(((u64)ss[1] << 32) | ss[0]), WS_BYTES);
    if (ss[14] == 0) st_flag(rs, s2.x, (((u64)ss[11] << 32) | ss[10]) + 64);
  }
}

__global__ void __launch_bounds__(256, 1) fused(Params p) {
  const int lane = threadIdx.x & 63, w = rfl(threadIdx.x >> 6);
  const int bid = blockIdx.x, x = bid & 7, k = bid >> 3;
  if (k >= 30) {
    u64 pc;
    asm volatile("s_getpc_b64 %0" : "=s"(pc));
    const unsigned c0 = __builtin_amdgcn_raw_buffer_load_b32(mkrs((const void*)pc, 56320), (((k & 1) * 4 + w) * 64 + lane) * 128, 0, 0);
    asm volatile("" ::"v"(c0));
  }
  rsrc_t rs = mkrs(p.ws, WS_BYTES);
  const u64 nonce = *(const __attribute__((address_space(4))) u64*)(p.ws + F_NONCE);
  if (w == 3) st_flag(rs, F_START + bid * 8, nonce + 1);
  if (x < 4) {
    if (k < 16) {
      role_chain(p, rs, nonce, x * 16 + k, lane, w);
      __builtin_amdgcn_endpgm();
    }
    {
      const int m = x * 16 + k - 16, r = (m & 1) * 256 + w * 64;
      __builtin_amdgcn_s_sleep(60);
      unsigned d[4 * 2];
#pragma unroll
      for (int q = 0; q < 4; q++) {
        const int mat = 4 == 4 ? q : q + 1;
        rsrc_t rw = mkrs(mat == 0 ? p.Wq : mat == 1 ? p.Wk : mat == 2 ? p.Wv : p.Wo, 512 * 4096 * 2);
#pragma unroll
        for (int u = 0; u < 2; u++)
          d[q * 2 + u] = __builtin_amdgcn_raw_buffer_load_b32(rw, mat < 3 ? (r + u * 32 + (lane >> 1)) * 8192 + (m >> 1) * 256 + (lane & 1) * 128
                                                                          : (m >> 1) * 131072 + (r * 2 + u * 64 + lane) * 128, 0, 0);
      }
#pragma unroll
      for (int q = 0; q < 4 * 2; q++) asm volatile("" ::"v"(d[q]));
    }
    if (bid == 128 && w == 1) {
      unsigned best[2] = {~0u, ~0u};
      for (int it = 0; it < 4; it++) {
  #pragma unroll
        for (int q = 0; q < 2; q++) {
          u64 t0 = __builtin_amdgcn_s_memtime();
          __builtin_amdgcn_raw_buffer_store_b32(0u, rs, lane == 0 ? 104 * 16384 + q * 8192 : OOB, 0, SC1);
          vmw<0>();
          u64 t1 = __builtin_amdgcn_s_memtime();
          best[q] = min(best[q], (unsigned)(t1 - t0));
        }
      }
      st_flag(rs, F_SEL, nonce + 1 + (best[1] < best[0] ? 1 : 0));
    }
    if (w == 0) {
      role_red(p, rs, nonce, x * 16 + k - 16, lane);
      __builtin_amdgcn_endpgm();
    }
  } else {
    const int t = k >> 3, hh = (x - 4) * 8 + (k & 7), j = (x - 4) + 4 * (k & 7);
    if (t >= 2) role_ln(p, rs, nonce, (x - 4) + 4 * (k - 16), lane, w);
    if (t == 1) {
      role_proj<true, false>(rs, mkrs(p.Wv, 512 * 8192), 8192, hh * 256, O_VX + hh * 8192, 262144, F_PROJ + (32 + hh) * 128, false, true, 0, 64, nonce, lane, w, 0 ? p.x : nullptr);
    } else if (t == 2) {
      role_proj<false, true>(rs, mkrs(p.Wq, 512 * 8192), 8192, (hh >> 1) * 512, O_QX + (hh >> 1) * 16384, 262144, F_PROJ + (64 + hh) * 128, false, true,
                             (hh & 1) * 32, (hh & 1) * 32 + 32, nonce, lane, w, nullptr);
    } else if (t < 3 || j < 22) {
      const bool ff = t == 3;
      const int cb = ff ? (j >= 11 ? j - 11 : j) : hh, s0 = ff && j >= 11 ? 32 : 0;
      const u16* wp = t == 0 ? p.Wk : p.w1;
      role_proj<false, false>(rs, mkrs(wp, ff ? 512 * 2752 : 512 * 8192), ff ? 2752 : 8192, cb * 256,
                       (t == 0 ? O_KX : O_HX) + cb * 8192, ff ? 90112 : 262144,
                       ff ? F_H + j * 8 : F_PROJ + (t * 32 + hh) * 128, ff, !(ff && cb == 10), s0, ff ? s0 + 32 : 64, nonce, lane, w, 0 && t == 0 ? p.x : nullptr);
    }
    auto* kp = (const __attribute__((address_space(4))) Params*)__builtin_amdgcn_kernarg_segment_ptr();
    asm volatile("" : "+s"(kp));
    const Params p2 = {kp->x, nullptr, nullptr, nullptr, nullptr, kp->Wo, nullptr, kp->w2, kp->out, nullptr};
    if (t == 3) role_final(p2, rs, nonce, hh, lane, w);
    else role_follow(p2, rs, nonce, hh, t, lane, w);
  }
}

extern "C" void kernel_launch(void* const* d_in, const int* in_sizes, int n_in, void* d_out, int out_size, void* d_ws,
                              size_t ws_size, hipStream_t stream) {
  Params p;
  p.x = (const u16*)d_in[0]; p.mem0 = (const u16*)d_in[1];
  p.Wq = (const u16*)d_in[2]; p.Wk = (const u16*)d_in[3]; p.Wv = (const u16*)d_in[4]; p.Wo = (const u16*)d_in[5];
  p.w1 = (const u16*)d_in[8]; p.w2 = (const u16*)d_in[9];
  p.out = (u16*)d_out;
  p.ws = (unsigned char*)(((uintptr_t)d_ws + (2u << 20) - 1) & ~(uintptr_t)((2u << 20) - 1));
  (void)hipFuncSetAttribute((const void*)fused, hipFuncAttributeMaxDynamicSharedMemorySize, 163840);
  fused<<<256, 256, 163840, stream>>>(p);
}
```
